```python
import torch

def _softmax(x, axis=-1):
    return torch.softmax(x, dim=axis)
def _gelu(x):
    return torch.nn.functional.gelu(x, approximate="tanh")
def _silu(x):
    return torch.nn.functional.silu(x)
def _sigmoid(x):
    return torch.sigmoid(x)
def _layernorm(x, axis=-1, eps=1e-5):
    m = x.mean(dim=axis, keepdim=True)
    v = x.var(dim=axis, keepdim=True, unbiased=False)
    return (x - m) / torch.sqrt(v + eps)
def _scatter_add(tgt, idx, src, axis=0):
    return tgt.clone().index_add_(axis, idx, src)
def _segment_sum(src, seg, n):
    out = torch.zeros((n,) + src.shape[1:], dtype=src.dtype)
    return out.index_add_(0, seg, src)
def _pad(x, pw):
    flat = tuple(v for pair in reversed(pw) for v in pair)
    return torch.nn.functional.pad(x, flat)

kernel_name = 'gated_mix__compressed_kv_cla__swiglu_ffn__decode'

def setup_inputs():

    _w = (lambda a: (lambda t: t.to(torch.bfloat16) if t.is_floating_point() else t)(torch.as_tensor(a)))
    def _A_setup():
        B, H, Sq, Sk, D, M = 2, 32, 1, 4096, 128, 640
        Dc = 4 * D
        return {
            "x": _w((lambda *s: torch.randn(*s, dtype=torch.bfloat16) * 0.05)(B, Sq, M)),
            "c_kv": _w((lambda *s: torch.randn(*s, dtype=torch.bfloat16) * 0.8)(B, Sk, Dc)),
            "w_q": _w((lambda *s: torch.randn(*s, dtype=torch.bfloat16) * 6.4)(M, H, D) * (M ** -0.5)),
            "w_uk": _w((lambda *s: torch.randn(*s, dtype=torch.bfloat16) * 6.4)(Dc, H, D) * (Dc ** -0.5)),
            "w_uv": _w((lambda *s: torch.randn(*s, dtype=torch.bfloat16) * 0.8)(Dc, H, D) * (Dc ** -0.5)),
            "w_o": _w((lambda *s: torch.randn(*s, dtype=torch.bfloat16) * 0.8)(H, D, M) * ((H * D) ** -0.5)),
        }
    _A_in = _A_setup()

    def _B_setup():
        B, S, D, F = 2, 1, 640, 1728
        return {
            "x": _w((lambda *s: torch.randn(*s, dtype=torch.bfloat16) * 0.05)(B, S, D)),
            "w_gate": _w((lambda *s: torch.randn(*s, dtype=torch.bfloat16) * 0.1)(D, F)),
            "w_up": _w((lambda *s: torch.randn(*s, dtype=torch.bfloat16) * 0.1)(D, F)),
            "w_down": _w((lambda *s: torch.randn(*s, dtype=torch.bfloat16) * 0.1)(F, D)),
        }
    _B_in = _B_setup()

    return {
        "A_x": _A_in["x"],
        "A_c_kv": _A_in["c_kv"],
        "A_w_q": _A_in["w_q"],
        "A_w_uk": _A_in["w_uk"],
        "A_w_uv": _A_in["w_uv"],
        "A_w_o": _A_in["w_o"],
        "B_x": _B_in["x"],
        "B_w_gate": _B_in["w_gate"],
        "B_w_up": _B_in["w_up"],
        "B_w_down": _B_in["w_down"],
        "gate_w": _w((lambda *s: torch.randn(*s, dtype=torch.bfloat16) * 0.1)(640) * 16.0),
    }

def reference(A_x, A_c_kv, A_w_q, A_w_uk, A_w_uv, A_w_o, B_x, B_w_gate, B_w_up, B_w_down, gate_w):
    def _A_ref(x, c_kv, w_q, w_uk, w_uv, w_o):
        B, Sq, M = x.shape[0], x.shape[1], x.shape[2]
        Sk = c_kv.shape[1]
        H, D = w_q.shape[1], w_q.shape[2]
        q = torch.einsum("bqm,mhd->bhqd", x, w_q)
        k = torch.einsum("bkc,chd->bhkd", c_kv, w_uk)
        v = torch.einsum("bkc,chd->bhkd", c_kv, w_uv)
        scores = torch.einsum("bhqd,bhkd->bhqk", q, k) * (D ** -0.5)
        off = Sk - Sq
        qi = torch.arange(Sq)[:, None]
        kj = torch.arange(Sk)[None, :]
        cmask = kj <= (qi + off)
        scores = torch.where(cmask[None, None], scores, -1e9)
        p = _softmax(scores, axis=-1)
        ctx = torch.einsum("bhqk,bhkd->bhqd", p, v)
        return torch.einsum("bhqd,hdm->bqm", ctx, w_o)

    def _B_ref(x, w_gate, w_up, w_down):
        g = _silu(x @ w_gate)
        u = x @ w_up
        return (g * u) @ w_down

    x = A_x
    g = _sigmoid((x * gate_w).sum(-1, keepdims=True))
    a = _A_ref(x, A_c_kv, A_w_q, A_w_uk, A_w_uv, A_w_o)
    b = _B_ref(x, B_w_gate, B_w_up, B_w_down)
    return g * a + (1 - g) * b
```

```cpp
#include <hip/hip_runtime.h>
#include <hip/hip_bf16.h>
#include <stdint.h>

typedef unsigned short u16;
__device__ __forceinline__ float bf2f(u16 b) { return __uint_as_float(((unsigned)b) << 16); }

constexpr int B = 2, H = 32, SK = 4096, D = 128, M = 640, DC = 512, F = 1728;

__global__ void k1(const u16* x, const u16* wq, const u16* wg, const u16* wu, float* q, float* hff) {
  __shared__ float xs[2][M];
  for (int i = threadIdx.x; i < 2 * M; i += blockDim.x) xs[i / M][i % M] = bf2f(x[i]);
  __syncthreads();
  int n = blockIdx.x * blockDim.x + threadIdx.x;
  if (n < H * D) {
    float a0 = 0, a1 = 0;
    for (int m = 0; m < M; ++m) { float w = bf2f(wq[(size_t)m * H * D + n]); a0 += xs[0][m] * w; a1 += xs[1][m] * w; }
    q[n] = a0; q[H * D + n] = a1;
  } else if (n < H * D + F) {
    int f = n - H * D;
    float g0 = 0, g1 = 0, u0 = 0, u1 = 0;
    for (int m = 0; m < M; ++m) {
      float w = bf2f(wg[(size_t)m * F + f]); float v = bf2f(wu[(size_t)m * F + f]);
      g0 += xs[0][m] * w; g1 += xs[1][m] * w; u0 += xs[0][m] * v; u1 += xs[1][m] * v;
    }
    hff[f] = g0 / (1.f + __expf(-g0)) * u0;
    hff[F + f] = g1 / (1.f + __expf(-g1)) * u1;
  }
}
__global__ void k2(const float* q, const u16* wuk, float* qabs) {
  int idx = blockIdx.x * blockDim.x + threadIdx.x;
  int c = idx % DC, h = (idx / DC) % H, b = idx / (DC * H);
  float a = 0;
  for (int d = 0; d < D; ++d) a += q[b * H * D + h * D + d] * bf2f(wuk[(size_t)c * H * D + h * D + d]);
  qabs[idx] = a * 0.08838834764831845f;
}
__global__ void k3(const float* qabs, const u16* ckv, float* s) {
  int idx = blockIdx.x * blockDim.x + threadIdx.x;
  int k = idx % SK, h = (idx / SK) % H, b = idx / (SK * H);
  float a = 0;
  for (int c = 0; c < DC; ++c) a += qabs[(b * H + h) * DC + c] * bf2f(ckv[((size_t)b * SK + k) * DC + c]);
  s[idx] = a;
}
__global__ void k4(float* s, const u16* ckv, float* lat) {
  __shared__ float p[SK];
  __shared__ float red[512];
  int bh = blockIdx.x, b = bh / H, t = threadIdx.x;
  float mx = -1e30f;
  for (int k = t; k < SK; k += 512) { p[k] = s[bh * SK + k]; mx = fmaxf(mx, p[k]); }
  red[t] = mx; __syncthreads();
  for (int o = 256; o > 0; o >>= 1) { if (t < o) red[t] = fmaxf(red[t], red[t + o]); __syncthreads(); }
  mx = red[0]; __syncthreads();
  float sm = 0;
  for (int k = t; k < SK; k += 512) { p[k] = __expf(p[k] - mx); sm += p[k]; }
  red[t] = sm; __syncthreads();
  for (int o = 256; o > 0; o >>= 1) { if (t < o) red[t] += red[t + o]; __syncthreads(); }
  sm = red[0];
  float a = 0;
  for (int k = 0; k < SK; ++k) a += p[k] * bf2f(ckv[((size_t)b * SK + k) * DC + t]);
  lat[bh * DC + t] = a / sm;
}
__global__ void k5(const float* lat, const u16* wuv, float* ctx) {
  int idx = blockIdx.x * blockDim.x + threadIdx.x;
  int d = idx % D, h = (idx / D) % H, b = idx / (D * H);
  float a = 0;
  for (int c = 0; c < DC; ++c) a += lat[(b * H + h) * DC + c] * bf2f(wuv[(size_t)c * H * D + h * D + d]);
  ctx[idx] = a;
}
__global__ void k6(const u16* x, const u16* gw, const float* ctx, const u16* wo, const float* hff, const u16* wd, __hip_bfloat16* out) {
  int idx = blockIdx.x * blockDim.x + threadIdx.x;
  if (idx >= B * M) return;
  int m = idx % M, b = idx / M;
  float gs = 0;
  for (int i = 0; i < M; ++i) gs += bf2f(x[b * M + i]) * bf2f(gw[i]);
  float g = 1.f / (1.f + __expf(-gs));
  float a = 0;
  for (int i = 0; i < H * D; ++i) a += ctx[b * H * D + i] * bf2f(wo[(size_t)i * M + m]);
  float f = 0;
  for (int i = 0; i < F; ++i) f += hff[b * F + i] * bf2f(wd[(size_t)i * M + m]);
  out[idx] = __float2bfloat16(g * a + (1.f - g) * f);
}

extern "C" void kernel_launch(void* const* d_in, const int* in_sizes, int n_in,
                              void* d_out, int out_size, void* d_ws, size_t ws_size,
                              hipStream_t stream) {
  const u16* x = (const u16*)d_in[0];
  const u16* ckv = (const u16*)d_in[1];
  const u16* wq = (const u16*)d_in[2];
  const u16* wuk = (const u16*)d_in[3];
  const u16* wuv = (const u16*)d_in[4];
  const u16* wo = (const u16*)d_in[5];
  const u16* wg = (const u16*)d_in[7];
  const u16* wu = (const u16*)d_in[8];
  const u16* wd = (const u16*)d_in[9];
  const u16* gw = (const u16*)d_in[10];
  float* ws = (float*)d_ws;
  float* q = ws; float* hff = q + B * H * D; float* qabs = hff + 4096; float* s = qabs + B * H * DC;
  float* lat = s + B * H * SK; float* ctx = lat + B * H * DC;
  k1<<<(H * D + F + 63) / 64, 64, 0, stream>>>(x, wq, wg, wu, q, hff);
  k2<<<B * H * DC / 256, 256, 0, stream>>>(q, wuk, qabs);
  k3<<<B * H * SK / 256, 256, 0, stream>>>(qabs, ckv, s);
  k4<<<B * H, 512, 0, stream>>>(s, ckv, lat);
  k5<<<B * H * D / 64, 64, 0, stream>>>(lat, wuv, ctx);
  k6<<<(B * M + 63) / 64, 64, 0, stream>>>(x, gw, ctx, wo, hff, wd, (__hip_bfloat16*)d_out);
}
```

```cpp
#include <hip/hip_runtime.h>
#include <stdint.h>

typedef unsigned u32;
typedef unsigned long long u64;
typedef unsigned short u16;
typedef u32 v4u __attribute__((ext_vector_type(4)));
typedef u32 v3u __attribute__((ext_vector_type(3)));
typedef u32 v2u __attribute__((ext_vector_type(2)));
typedef float f4 __attribute__((ext_vector_type(4)));
typedef __bf16 bf8 __attribute__((ext_vector_type(8)));
typedef __bf16 bf2 __attribute__((ext_vector_type(2)));
typedef float f2 __attribute__((ext_vector_type(2)));
typedef short s4 __attribute__((ext_vector_type(4)));
#define GAS __attribute__((address_space(1)))
#define LAS __attribute__((address_space(3)))
typedef __amdgpu_buffer_rsrc_t rsrc_t;

constexpr int AUX_NT = 2, AUX_SC1 = 16;
#define WPOL 0
#define CODE_BYTES 15360
constexpr u32 SPIN_CAP = 1u << 17;
constexpr u32 SENT = 0xFFFFFFFFu;

constexpr int WS_EPOCH = 0;
constexpr int WS_ML = 1024;
constexpr int WS_QP = 65536;
constexpr int WS_QA = WS_QP + 256 * 1024;
constexpr int WS_FF = WS_QA + 128 * 1024;
constexpr int WS_FA = WS_FF + 108 * 5120;
constexpr int WS_OB = 2 * 1024 * 1024;
constexpr int WS_END = WS_OB + 2 * 1024 * 1024;
static_assert(WS_FA % 128 == 0 && WS_FA + 40 * 128 * 128 <= WS_OB, "");

constexpr int RS = 1056;
constexpr int L2_QF = 128 * RS;
constexpr int L2_PF = L2_QF + 16 * RS;
constexpr int L2_MX = L2_PF + 4096;
constexpr int L2_LS = L2_MX + 512;
constexpr int L2_FR = L2_LS + 512;
constexpr int L2_AR = L2_FR + 1024;
constexpr int L2_H = L2_AR + 1024;
constexpr int LDS_BYTES = L2_H + 64;
constexpr int L1_UV = 0, L1_WO = 32768, L1_UK = L1_WO, L1_RED = L1_WO + 81920, L1_LA = L1_RED + 8192, L1_CX = L1_LA + 2048, L1_Q = L1_CX + 8 * 144, L1_W = L1_Q + 576;
static_assert(L1_Q + 1024 <= LDS_BYTES && LDS_BYTES <= 163840, "");

__device__ __forceinline__ f4 mfma(v4u a, v4u b, f4 c) {
  return __builtin_amdgcn_mfma_f32_16x16x32_bf16(__builtin_bit_cast(bf8, a), __builtin_bit_cast(bf8, b), c, 0, 0, 0);
}
__device__ __forceinline__ u32 pk(float lo, float hi) {
  f2 v = {lo, hi};
  return __builtin_bit_cast(u32, __builtin_convertvector(v, bf2));
}
__device__ __forceinline__ float blo(u32 v) { return __uint_as_float(v << 16); }
__device__ __forceinline__ float bhi(u32 v) { return __uint_as_float(v & 0xFFFF0000u); }
template <int CTRL> __device__ __forceinline__ float dpp(float x) {
  return __builtin_bit_cast(float, __builtin_amdgcn_mov_dpp(__builtin_bit_cast(int, x), CTRL, 0xf, 0xf, true));
}
constexpr int XOR1 = 0xB1, XOR2 = 0x4E, ROR8 = 0x128, ROR4 = 0x124;
__device__ __forceinline__ float xrow_max(float x) {
  auto s = __builtin_amdgcn_permlane16_swap(__float_as_uint(x), __float_as_uint(x), false, false);
  x = fmaxf(__uint_as_float(s[0]), __uint_as_float(s[1]));
  auto t = __builtin_amdgcn_permlane32_swap(__float_as_uint(x), __float_as_uint(x), false, false);
  return fmaxf(__uint_as_float(t[0]), __uint_as_float(t[1]));
}
__device__ __forceinline__ float xrow_sum(float x) {
  auto s = __builtin_amdgcn_permlane16_swap(__float_as_uint(x), __float_as_uint(x), false, false);
  x = __uint_as_float(s[0]) + __uint_as_float(s[1]);
  auto t = __builtin_amdgcn_permlane32_swap(__float_as_uint(x), __float_as_uint(x), false, false);
  return __uint_as_float(t[0]) + __uint_as_float(t[1]);
}
__device__ __forceinline__ rsrc_t mk(const void* p, u32 bytes) { return __builtin_amdgcn_make_buffer_rsrc((void*)p, 0, bytes, 0x00020000); }
__device__ __forceinline__ v4u ldw(rsrc_t r, int vo, int so) { return __builtin_amdgcn_raw_buffer_load_b128(r, vo, so, WPOL); }
__device__ __forceinline__ v4u ldc(rsrc_t r, int vo, int so) { return __builtin_amdgcn_raw_buffer_load_b128(r, vo, so, AUX_SC1); }
__device__ __forceinline__ void stc(v4u v, rsrc_t r, int vo) { __builtin_amdgcn_raw_buffer_store_b128(v, r, vo, 0, AUX_SC1); }
__device__ __forceinline__ void stc2(v2u v, rsrc_t r, int vo, int so) { __builtin_amdgcn_raw_buffer_store_b64(v, r, vo, so, AUX_SC1); }
__device__ __forceinline__ void stc1(u32 v, rsrc_t r, int vo, int so) { __builtin_amdgcn_raw_buffer_store_b32(v, r, vo, so, AUX_SC1); }
__device__ __forceinline__ void dma(rsrc_t r, LAS unsigned char* l, int vo, int so) {
  __builtin_amdgcn_raw_ptr_buffer_load_lds(r, (LAS void*)l, 16, vo, so, 0, WPOL);
}
__device__ __forceinline__ v2u trd(LAS unsigned char* p) {
  return __builtin_bit_cast(v2u, __builtin_amdgcn_ds_read_tr16_b64_v4i16((LAS s4*)p));
}
__device__ __forceinline__ void bar() {
  asm volatile("s_waitcnt lgkmcnt(0)" ::: "memory");
  __builtin_amdgcn_s_barrier();
  asm volatile("" ::: "memory");
}
#define PSLEEP 0
#define POLL() do { if (PSLEEP) __builtin_amdgcn_s_sleep(PSLEEP); asm volatile("" ::: "memory"); } while (0)
#define G1_DELAY 0
#define G2_DELAY 0
#define FFN_POS 0
__device__ __forceinline__ void delay(int ticks) {
  if (ticks) {
    const u64 t0 = __builtin_amdgcn_s_memrealtime();
    while (__builtin_amdgcn_s_memrealtime() < t0 + ticks) __builtin_amdgcn_s_sleep(1);
  }
}
#define DRAIN() asm volatile("s_waitcnt vmcnt(0)" ::: "memory")
#define SCHED() __builtin_amdgcn_sched_barrier(0)
__device__ __forceinline__ bool anyset(u32 bad) { return __builtin_amdgcn_ballot_w64(bad != 0u) != 0ull; }
__device__ __forceinline__ u32 tagbad(v4u a, u32 tag) { return (a.y ^ tag) | (a.w ^ tag); }
__device__ __forceinline__ bool sentbad(v4u a) { return __builtin_amdgcn_ballot_w64(max(max(a.x, a.y), max(a.z, a.w)) == SENT) != 0ull; }
__device__ __forceinline__ v4u selp(u32 v, int n) {
  const int q = (n >> 1) & 3;
  return v4u{q == 0 ? v : 0u, q == 1 ? v : 0u, q == 2 ? v : 0u, q == 3 ? v : 0u};
}
__device__ __forceinline__ u32 plo(u32 d, int n) { return __builtin_amdgcn_perm(0u, d, (n & 1) ? 0x01000c0cu : 0x0c0c0100u); }
__device__ __forceinline__ u32 phi(u32 d, int n) { return __builtin_amdgcn_perm(0u, d, (n & 1) ? 0x03020c0cu : 0x0c0c0302u); }
__device__ __forceinline__ f4 gemv5(v3u xd, const v4u (&wv)[5], int n, int g) {
  const u32 sh = (g & 1) << 4;
  const u32 a0 = __builtin_amdgcn_alignbit(xd.y, xd.x, sh), a1 = __builtin_amdgcn_alignbit(xd.z, xd.y, sh), a2 = xd.z >> sh;
  const v4u s0 = selp(plo(a0, n), n), s1 = selp(phi(a0, n), n), s2 = selp(plo(a1, n), n), s3 = selp(phi(a1, n), n), s4_ = selp(plo(a2, n), n);
  f4 acc = {0.f, 0.f, 0.f, 0.f};
  acc = mfma(s0, wv[0], acc);
  acc = mfma(s1, wv[1], acc);
  acc = mfma(s2, wv[2], acc);
  acc = mfma(s3, wv[3], acc);
  acc = mfma(s4_, wv[4], acc);
  return acc;
}
__device__ __forceinline__ f4 unpk(v4u a) { return f4{blo(a.x), bhi(a.x), blo(a.z), bhi(a.z)}; }

#define ST(i)

extern "C" __global__ void __launch_bounds__(512, 2)
mla_ffn_mix(const u16* __restrict__ x, const u16* __restrict__ ckv, const u16* __restrict__ wq, const u16* __restrict__ wuk,
            const u16* __restrict__ wuv, const u16* __restrict__ wo, const u16* __restrict__ wg, const u16* __restrict__ wu,
            const u16* __restrict__ wd, const u16* __restrict__ gw, u16* __restrict__ out, unsigned char* __restrict__ ws) {
  __shared__ __attribute__((aligned(16))) unsigned char smem[LDS_BYTES];
  LAS unsigned char* L = (LAS unsigned char*)smem;
  ST(0);
  const int tid = threadIdx.x, lane = tid & 63, w = __builtin_amdgcn_readfirstlane(tid >> 6);
  const int bid = blockIdx.x, xcd = bid & 7, r = bid >> 3;
  const int n = lane & 15, g = lane >> 4;
  u32 pf_ = 0;
  if (w < 2) {
    u64 pc;
    asm volatile("s_getpc_b64 %0" : "=s"(pc));
    const GAS unsigned char* cb = (const GAS unsigned char*)(pc & ~255ull);
    pf_ = *(const GAS u32*)(cb + min(tid * 128, CODE_BYTES - 128));
  }
  asm volatile("" ::"s"(x), "s"(ckv), "s"(wq), "s"(wuk), "s"(wuv), "s"(wo), "s"(wg), "s"(wu), "s"(wd), "s"(gw), "s"(out), "s"(ws));
  const rsrc_t rws = mk(ws, WS_END);

  if (__builtin_expect(r < 16, 1)) {
    const int h = 4 * xcd + (r >> 2), i = r & 3;
    v4u wv[10];
    u32 tag;
    if (w < 4) {
      const rsrc_t rx = mk(x, 2560), rq = mk(wq, 640 * 8192);
      const int row = 160 * i + 40 * w + 10 * g;
      const v4u xa = __builtin_amdgcn_raw_buffer_load_b128(rx, ((n >> 3) * 640 + row) * 2, 0, 0);
      const u32 xb = __builtin_amdgcn_raw_buffer_load_b32(rx, ((n >> 3) * 640 + row) * 2 + 16, 0, 0);
#pragma unroll
      for (int t = 0; t < 10; ++t) wv[t] = ldw(rq, row * 8192 + h * 256 + n * 16, t * 8192);
      tag = __builtin_amdgcn_raw_buffer_load_b32(rws, 0, WS_EPOCH, 0);
      SCHED();
      ST(1);
      f4 acc = {0.f, 0.f, 0.f, 0.f};
#pragma unroll
      for (int t = 0; t < 5; ++t) {
        const u32 xx = t < 4 ? xa[t & 3] : xb;
        acc = mfma(selp(plo(xx, n), n), wv[2 * t], acc);
        acc = mfma(selp(phi(xx, n), n), wv[2 * t + 1], acc);
      }
      *(LAS f4*)(L + L1_RED + w * 1024 + lane * 16) = acc;
      ST(2);
      bar();
      tag += 1u;
      if (w == 0) {
        f4 s = (acc + *(LAS f4*)(L + L1_RED + 1024 + lane * 16)) + (*(LAS f4*)(L + L1_RED + 2048 + lane * 16) + *(LAS f4*)(L + L1_RED + 3072 + lane * 16));
        stc(v4u{pk(s[0], s[1]), tag, pk(s[2], s[3]), tag}, rws, WS_QP + (h * 4 + i) * 1024 + lane * 16);
        ST(3);
        v4u q1, q2, q3;
        for (u32 it = 0; it < SPIN_CAP; ++it) {
          POLL();
          q1 = ldc(rws, lane * 16, WS_QP + (h * 4 + (i ^ 1)) * 1024);
          q2 = ldc(rws, lane * 16, WS_QP + (h * 4 + (i ^ 2)) * 1024);
          q3 = ldc(rws, lane * 16, WS_QP + (h * 4 + (i ^ 3)) * 1024);
          if (!anyset(tagbad(q1, tag) | tagbad(q2, tag) | tagbad(q3, tag))) break;
        }
        s = (s + unpk(q1)) + (unpk(q2) + unpk(q3));
        *(LAS v2u*)(L + L1_Q + (g >> 1) * 272 + n * 16 + (g & 1) * 8) = v2u{pk(s[0], s[1]), pk(s[2], s[3])};
        ST(4);
      }
      bar();
    } else {
      const rsrc_t rk = mk(wuk, 512 * 8192);
      tag = __builtin_amdgcn_raw_buffer_load_b32(rws, 0, WS_EPOCH, 0);
#pragma unroll
      for (int k = 0; k < 8; ++k) wv[k] = ldw(rk, g * 8192 + n * 16, (128 * i + 32 * (w - 4) + 4 * k) * 8192 + h * 256);
      SCHED();
      ST(1);
      __builtin_amdgcn_s_barrier();
      tag += 1u;
      LAS unsigned char* U = L + L1_UK + (w - 4) * 8704;
#pragma unroll
      for (int k = 0; k < 8; ++k) *(LAS v4u*)(U + (4 * k + g) * 272 + n * 16) = wv[k];
#pragma unroll
      for (int k = 0; k < 8; ++k) wv[k] = *(LAS v4u*)(U + (2 * n + (k >> 2)) * 272 + (k & 3) * 64 + g * 16);
      ST(2);
      bar();
      f4 a0 = {0.f, 0.f, 0.f, 0.f}, a1 = {0.f, 0.f, 0.f, 0.f};
#pragma unroll
      for (int s = 0; s < 4; ++s) {
        const v4u qf = *(LAS v4u*)(L + L1_Q + (n & 1) * 272 + s * 64 + g * 16);
        a0 = mfma(qf, wv[s], a0);
        a1 = mfma(qf, wv[4 + s], a1);
      }
      constexpr float SC = 0.08838834764831845f * 1.4426950408889634f;
      const float v0 = ((g & 1) ? a0[1] : a0[0]) * SC, v1 = ((g & 1) ? a1[1] : a1[0]) * SC;
      if (lane < 32) stc2(v2u{pk(v0, v1), tag}, rws, g * 65536 + n * 8, WS_QA + h * 2048 + i * 512 + (w - 4) * 128);
    }
    SCHED();
    __builtin_amdgcn_s_barrier();
    ST(5);
    delay(G1_DELAY);
    const int dh = i >> 1, ch = i & 1;
    {
      const rsrc_t rv = mk(wuv, 512 * 8192), ro = mk(wo, 4096 * 1280);
      const int lr = lane >> 3;
#pragma unroll
      for (int k = 0; k < 4; ++k)
        dma(rv, L + L1_UV + (4 * w + k) * 1024, lr * 8192 + (((lane & 7) ^ (((2 * w + (k >> 1)) & 3) << 1)) << 4),
            (256 * ch + 32 * w + 8 * k) * 8192 + h * 256 + dh * 128);
      const int ck = (lane & 7) ^ (((lr >> 1) & 3) << 1);
#pragma unroll
      for (int k = 0; k < 10; ++k) {
        const int blk = k + 5 * ch >= 10 ? k + 5 * ch - 10 : k + 5 * ch;
        dma(ro, L + L1_WO + blk * 8192 + w * 1024, lr * 1280 + ck * 16, (h * 128 + 64 * dh + 8 * w) * 1280 + blk * 128);
      }
      SCHED();
    }
    ST(6);
    const int b = w >> 2, cb = w & 3;
    v4u ov[4];
    v2u fv[16], fo[20];
    {
      DRAIN();
      ST(7);
      bar();
      {
        const int q = n >> 2, p = n & 3;
        const int rbv = (16 * q + g) * 128 + p * 8;
#pragma unroll
        for (int t = 0; t < 4; ++t)
#pragma unroll
          for (int e = 0; e < 4; ++e) fv[t * 4 + e] = trd(L + L1_UV + cb * 8192 + ((e & 1) * 8 + (e >> 1) * 4) * 128 + rbv + ((t ^ q) << 5));
        const int sw = 2 * (g & 1) + (q >> 1), rb = (4 * g + q) * 128 + p * 8;
#pragma unroll
        for (int t = 0; t < 5; ++t) {
          const int T = 5 * w + t;
#pragma unroll
          for (int e = 0; e < 4; ++e) fo[t * 4 + e] = trd(L + L1_WO + (T >> 2) * 8192 + e * 2048 + rb + (((T & 3) ^ sw) << 5));
        }
      }
      v4u ml;
      if (w == 0) {
        for (u32 it = 0; it < SPIN_CAP; ++it) {
          POLL();
          ml = ldc(rws, lane * 512, WS_ML + h * 16);
          if (!anyset(tagbad(ml, tag))) break;
        }
        ST(11);
      }
      SCHED();
      __builtin_amdgcn_s_barrier();
      asm volatile("" ::: "memory");
      const int kg0 = 8 * g + 4 * (n >> 3);
      const int vo = kg0 * 1024 + (n & 7) * 16, so = WS_OB + (b * 32 + h) * 32768 + ch * 512 + cb * 128;
#pragma unroll
      for (int t = 0; t < 4; ++t) ov[t] = ldc(rws, vo + t * 1024, so);
      SCHED();
      if (w == 0) {
        const float m = __uint_as_float(ml.x);
        float M = fmaxf(m, dpp<XOR1>(m));
        M = fmaxf(M, dpp<XOR2>(M));
        M = fmaxf(M, dpp<ROR4>(M));
        M = fmaxf(M, dpp<ROR8>(M));
        auto sm = __builtin_amdgcn_permlane16_swap(__float_as_uint(M), __float_as_uint(M), false, false);
        M = fmaxf(__uint_as_float(sm[0]), __uint_as_float(sm[1]));
        const float e = __builtin_amdgcn_exp2f(m - M);
        const u32 wb = pk(e, e);
        *(LAS u16*)(L + L1_W + lane * 2) = (u16)wb;
        float Ls = blo(wb) * __uint_as_float(ml.z);
        Ls += dpp<XOR1>(Ls);
        Ls += dpp<XOR2>(Ls);
        Ls += dpp<ROR4>(Ls);
        Ls += dpp<ROR8>(Ls);
        auto sl = __builtin_amdgcn_permlane16_swap(__float_as_uint(Ls), __float_as_uint(Ls), false, false);
        *(LAS float*)(L + L1_W + 128 + (lane >> 5) * 4) = __builtin_amdgcn_rcpf(__uint_as_float(sl[0]) + __uint_as_float(sl[1]));
      }
      bar();
      const v2u w4 = *(LAS v2u*)(L + L1_W + b * 64 + kg0 * 2);
      const float inv = *(LAS float*)(L + L1_W + 128 + b * 4);
      const v4u aw0 = selp(plo(w4.x, n), n), aw1 = selp(phi(w4.x, n), n), aw2 = selp(plo(w4.y, n), n), aw3 = selp(phi(w4.y, n), n);
      SCHED();
      for (u32 it = 0; it < SPIN_CAP; ++it) {
        const bool b0 = sentbad(ov[0]), b1 = sentbad(ov[1]), b2 = sentbad(ov[2]), b3 = sentbad(ov[3]);
        if (!(b0 || b1 || b2 || b3)) break;
        POLL();
        if (b0) ov[0] = ldc(rws, vo, so);
        if (b1) ov[1] = ldc(rws, vo + 1024, so);
        if (b2) ov[2] = ldc(rws, vo + 2048, so);
        if (b3) ov[3] = ldc(rws, vo + 3072, so);
      }
      ST(8);
      f4 acc = {0.f, 0.f, 0.f, 0.f};
      acc = mfma(aw0, ov[0], acc);
      acc = mfma(aw1, ov[1], acc);
      acc = mfma(aw2, ov[2], acc);
      acc = mfma(aw3, ov[3], acc);
      if ((g >> 1) == (n >> 3))
        *(LAS v2u*)(L + L1_LA + w * 256 + (g >> 1) * 128 + (n & 7) * 16 + (g & 1) * 8) = v2u{pk(acc[0] * inv, acc[1] * inv), pk(acc[2] * inv, acc[3] * inv)};
    }
    {
      const v4u a0 = *(LAS v4u*)(L + L1_LA + w * 256 + (n & 1) * 128 + g * 16), a1 = *(LAS v4u*)(L + L1_LA + w * 256 + (n & 1) * 128 + 64 + g * 16);
      float cv[4];
#pragma unroll
      for (int t = 0; t < 4; ++t) {
        f4 d = {0.f, 0.f, 0.f, 0.f};
        d = mfma(a0, v4u{fv[t * 4].x, fv[t * 4].y, fv[t * 4 + 1].x, fv[t * 4 + 1].y}, d);
        d = mfma(a1, v4u{fv[t * 4 + 2].x, fv[t * 4 + 2].y, fv[t * 4 + 3].x, fv[t * 4 + 3].y}, d);
        cv[t] = d[0] + d[1];
      }
      const float v01 = (g & 1) ? cv[1] : cv[0], v23 = (g & 1) ? cv[3] : cv[2], v = (g & 2) ? v23 : v01;
      *(LAS u16*)(L + L1_CX + w * 144 + (32 * (g >> 1) + 8 * (n >> 2) + 4 * (g & 1) + (n & 3)) * 2) = (u16)pk(v, v);
    }
    bar();
    ST(9);
    {
      const v4u a0 = *(LAS v4u*)(L + L1_CX + (n & 7) * 144 + g * 16), a1 = *(LAS v4u*)(L + L1_CX + (n & 7) * 144 + 64 + g * 16);
      u32 cv[5];
#pragma unroll
      for (int t = 0; t < 5; ++t) {
        f4 d = {0.f, 0.f, 0.f, 0.f};
        d = mfma(a0, v4u{fo[t * 4].x, fo[t * 4].y, fo[t * 4 + 1].x, fo[t * 4 + 1].y}, d);
        d = mfma(a1, v4u{fo[t * 4 + 2].x, fo[t * 4 + 2].y, fo[t * 4 + 3].x, fo[t * 4 + 3].y}, d);
        const u32 sv = __float_as_uint((d[0] + d[1]) + (d[2] + d[3]));
        auto sp = __builtin_amdgcn_permlane16_swap(sv, sv, false, false);
        cv[t] = pk(__uint_as_float(sp[0]), __uint_as_float(sp[1]));
      }
      const int so = WS_FA + 5 * w * 16384 + (h * 4 + i) * 128;
      const u32 c01 = (g & 1) ? cv[1] : cv[0], c23 = (g & 1) ? cv[3] : cv[2];
      stc2(v2u{(g & 2) ? c23 : c01, tag}, rws, g * 16384 + n * 8, so);
      if (lane < 16) stc2(v2u{cv[4], tag}, rws, n * 8, so + 4 * 16384);
    }
    ST(10);
  } else {
    const int u = xcd * 16 + (r - 16), hg = r & 1, bk = u >> 1, b = bk >> 5, kg = bk & 31;
    u32 tag = __builtin_amdgcn_raw_buffer_load_b32(rws, 0, WS_EPOCH, 0);
    const int o_vo = WS_OB + (b * 32 + 16 * hg + 4 * g + 2 * (n >> 3)) * 32768 + kg * 1024 + w * 128 + (n & 7) * 16;
    stc(v4u{SENT, SENT, SENT, SENT}, rws, o_vo);
    stc(v4u{SENT, SENT, SENT, SENT}, rws, o_vo + 32768);
    delay(G2_DELAY);
    ST(1);
    {
      const rsrc_t rc = mk(ckv, 2 * 4096 * 1024);
#pragma unroll
      for (int k = 0; k < 16; ++k) {
        const int row = 16 * w + ((k + 8 * hg) & 15);
        dma(rc, L + row * RS, lane * 16, (bk * 128 + row) * 1024);
      }
      SCHED();
    }
    ST(2);
    const int fs = u >> 2, mq = u & 3;
    const int frow = 160 * mq + 20 * w + 5 * g;
    float gate = 0.f;
    v3u xd;
    {
      const rsrc_t rx = mk(x, 2560), rg = mk(gw, 1280);
      xd = __builtin_amdgcn_raw_buffer_load_b96(rx, (((n >> 3) * 640 + frow) * 2) & ~3, 0, 0);
      if (w == 0 && u >= 88) {
        float s = 0.f;
#pragma unroll
        for (int k = 0; k < 5; ++k) {
          const v2u a = __builtin_amdgcn_raw_buffer_load_b64(rx, (lane >> 5) * 1280 + (lane & 31) * 8 + k * 256, 0, 0);
          const v2u c = __builtin_amdgcn_raw_buffer_load_b64(rg, (lane & 31) * 8 + k * 256, 0, 0);
          s += (blo(a.x) * blo(c.x) + bhi(a.x) * bhi(c.x)) + (blo(a.y) * blo(c.y) + bhi(a.y) * bhi(c.y));
        }
        s += dpp<XOR1>(s);
        s += dpp<XOR2>(s);
        s += dpp<ROR4>(s);
        s += dpp<ROR8>(s);
        auto sp = __builtin_amdgcn_permlane16_swap(__float_as_uint(s), __float_as_uint(s), false, false);
        s = __uint_as_float(sp[0]) + __uint_as_float(sp[1]);
        gate = 1.f / (1.f + __expf(-s));
      }
    }
    DRAIN();
    ST(3);
    tag += 1u;
    v4u kf[16];
#pragma unroll
    for (int s = 0; s < 16; ++s) kf[s] = *(LAS v4u*)(L + (16 * w + n) * RS + s * 64 + g * 16);
    bar();
    v2u vf[32];
    {
      v4u qa[4];
      const int so = WS_QA + (b * 32 + 16 * hg + 2 * w) * 2048;
#pragma unroll
      for (int k = 0; k < 4; ++k) qa[k] = ldc(rws, lane * 16 + k * 1024, so);
      SCHED();
      {
        const int rb = (4 * g + (n >> 2)) * RS + w * 128 + (n & 3) * 8;
#pragma unroll
        for (int t = 0; t < 4; ++t)
#pragma unroll
          for (int e = 0; e < 8; ++e) vf[t * 8 + e] = trd(L + rb + e * 16 * RS + t * 32);
      }
      SCHED();
      ST(4);
      for (u32 it = 0; it < SPIN_CAP; ++it) {
        const bool b0 = anyset(tagbad(qa[0], tag)), b1 = anyset(tagbad(qa[1], tag)), b2 = anyset(tagbad(qa[2], tag)), b3 = anyset(tagbad(qa[3], tag));
        if (!(b0 || b1 || b2 || b3)) break;
        POLL();
        if (b0) qa[0] = ldc(rws, lane * 16, so);
        if (b1) qa[1] = ldc(rws, lane * 16 + 1024, so);
        if (b2) qa[2] = ldc(rws, lane * 16 + 2048, so);
        if (b3) qa[3] = ldc(rws, lane * 16 + 3072, so);
      }
#pragma unroll
      for (int k = 0; k < 4; ++k) *(LAS v2u*)(L + L2_QF + (2 * w + (k >> 1)) * RS + (k & 1) * 512 + lane * 8) = v2u{qa[k].x, qa[k].z};
    }
    ST(5);
    bar();
    v4u wv[5], dv[4];
    f4 sa = {0.f, 0.f, 0.f, 0.f};
    {
      const GAS unsigned char* wp = (const GAS unsigned char*)(n < 8 ? wg : wu) + frow * 3456 + (fs < 27 ? fs : 0) * 128 + (n & 7) * 16;
      LAS unsigned char* Q = L + L2_QF + n * RS + g * 16;
      v4u qf[16];
#pragma unroll
      for (int s = 0; s < 8; ++s) qf[s] = *(LAS v4u*)(Q + s * 64);
      SCHED();
#pragma unroll
      for (int s = 0; s < 16; ++s) {
        sa = mfma(kf[s], qf[s], sa);
        if (s < 8) qf[s + 8] = *(LAS v4u*)(Q + (s + 8) * 64);
        if (FFN_POS == 0 && s % 3 == 0 && s < 15) wv[s / 3] = *(const GAS v4u*)(wp + (s / 3) * 3456);
        SCHED();
      }
    }
    {
      const float m = xrow_max(fmaxf(fmaxf(sa[0], sa[1]), fmaxf(sa[2], sa[3])));
      if (g == 0) *(LAS float*)(L + L2_MX + n * 32 + w * 4) = m;
    }
    ST(6);
    bar();
    float M;
    {
      const f4 m0 = *(LAS f4*)(L + L2_MX + n * 32), m1 = *(LAS f4*)(L + L2_MX + n * 32 + 16);
      M = fmaxf(fmaxf(fmaxf(m0[0], m0[1]), fmaxf(m0[2], m0[3])), fmaxf(fmaxf(m1[0], m1[1]), fmaxf(m1[2], m1[3])));
      const u32 p01 = pk(__builtin_amdgcn_exp2f(sa[0] - M), __builtin_amdgcn_exp2f(sa[1] - M));
      const u32 p23 = pk(__builtin_amdgcn_exp2f(sa[2] - M), __builtin_amdgcn_exp2f(sa[3] - M));
      *(LAS v2u*)(L + L2_PF + (w >> 1) * 1024 + lane * 16 + (w & 1) * 8) = v2u{p01, p23};
      const float l = xrow_sum((blo(p01) + bhi(p01)) + (blo(p23) + bhi(p23)));
      if (g == 0) *(LAS float*)(L + L2_LS + n * 32 + w * 4) = l;
    }
    bar();
    ST(7);
    {
      v4u pf[4];
#pragma unroll
      for (int s = 0; s < 4; ++s) pf[s] = *(LAS v4u*)(L + L2_PF + s * 1024 + lane * 16);
      if (w == 0) {
        const f4 l0 = *(LAS f4*)(L + L2_LS + n * 32), l1 = *(LAS f4*)(L + L2_LS + n * 32 + 16);
        const float l = ((l0[0] + l0[1]) + (l0[2] + l0[3])) + ((l1[0] + l1[1]) + (l1[2] + l1[3]));
        if (lane < 16) stc(v4u{__float_as_uint(M), tag, __float_as_uint(l), tag}, rws, WS_ML + bk * 512 + (16 * hg + n) * 16);
      }
      f4 o[4];
#pragma unroll
      for (int t = 0; t < 4; ++t) o[t] = f4{0.f, 0.f, 0.f, 0.f};
#pragma unroll
      for (int s = 0; s < 4; ++s)
#pragma unroll
        for (int t = 0; t < 4; ++t)
          o[t] = mfma(pf[s], v4u{vf[t * 8 + 2 * s].x, vf[t * 8 + 2 * s].y, vf[t * 8 + 2 * s + 1].x, vf[t * 8 + 2 * s + 1].y}, o[t]);
      u32 pa[4], pb[4];
#pragma unroll
      for (int k = 0; k < 4; ++k) {
        pa[k] = min(pk(o[0][k], o[1][k]), SENT - 1u);
        pb[k] = min(pk(o[2][k], o[3][k]), SENT - 1u);
      }
#pragma unroll
      for (int k = 0; k < 2; ++k) {
        v4u pc;
        pc.x = __builtin_amdgcn_update_dpp(pa[k], pa[2 + k], ROR8, 0xf, 0xc, false);
        pc.y = __builtin_amdgcn_update_dpp(pb[k], pb[2 + k], ROR8, 0xf, 0xc, false);
        pc.z = __builtin_amdgcn_update_dpp(pa[2 + k], pa[k], ROR8, 0xf, 0x3, false);
        pc.w = __builtin_amdgcn_update_dpp(pb[2 + k], pb[k], ROR8, 0xf, 0x3, false);
        stc(pc, rws, o_vo + k * 32768);
      }
    }
    ST(8);
    if (u < 108) {
      SCHED();
      __builtin_amdgcn_s_barrier();
      if (FFN_POS == 1) {
        const GAS unsigned char* wp = (const GAS unsigned char*)(n < 8 ? wg : wu) + frow * 3456 + fs * 128 + (n & 7) * 16;
#pragma unroll
        for (int t = 0; t < 5; ++t) wv[t] = *(const GAS v4u*)(wp + t * 3456);
        SCHED();
      }
      if (w < 5) {
        const rsrc_t rd = mk(wd, 1728 * 1280);
#pragma unroll
        for (int t = 0; t < 4; ++t) dv[t] = ldw(rd, g * 4 * 1280 + n * 16 + t * 1280, (64 * fs + 16 * mq) * 1280 + w * 256);
        SCHED();
      }
      *(LAS f4*)(L + L2_QF + w * 1024 + lane * 16) = gemv5(xd, wv, n, g);
      bar();
      if (w == 7) {
        f4 s = *(LAS f4*)(L + L2_QF + lane * 16);
#pragma unroll
        for (int k = 1; k < 8; ++k) s += *(LAS f4*)(L + L2_QF + k * 1024 + lane * 16);
        stc(v4u{pk(s[0], s[1]), tag, pk(s[2], s[3]), tag}, rws, WS_QP + (128 + u) * 1024 + lane * 16);
        ST(9);
        v4u q1, q2, q3;
        for (u32 it = 0; it < SPIN_CAP; ++it) {
          POLL();
          q1 = ldc(rws, lane * 16, WS_QP + (128 + (u ^ 1)) * 1024);
          q2 = ldc(rws, lane * 16, WS_QP + (128 + (u ^ 2)) * 1024);
          q3 = ldc(rws, lane * 16, WS_QP + (128 + (u ^ 3)) * 1024);
          if (!anyset(tagbad(q1, tag) | tagbad(q2, tag) | tagbad(q3, tag))) break;
        }
        s = (s + unpk(q1)) + (unpk(q2) + unpk(q3));
        float hv[4];
#pragma unroll
        for (int k = 0; k < 4; ++k) hv[k] = s[k] * dpp<ROR8>(s[k]) * __builtin_amdgcn_rcpf(1.f + __expf(-s[k]));
        if ((n >> 1) == mq) *(LAS v2u*)(L + L2_H + (g >> 1) * 32 + (n & 1) * 16 + (g & 1) * 8) = v2u{pk(hv[0], hv[1]), pk(hv[2], hv[3])};
        ST(10);
      }
      bar();
      if (w < 5) {
        const v2u hh = *(LAS v2u*)(L + L2_H + (n >> 3) * 32 + g * 8);
        f4 d = {0.f, 0.f, 0.f, 0.f};
        d = mfma(selp(plo(hh.x, n), n), dv[0], d);
        d = mfma(selp(phi(hh.x, n), n), dv[1], d);
        d = mfma(selp(plo(hh.y, n), n), dv[2], d);
        d = mfma(selp(phi(hh.y, n), n), dv[3], d);
        stc(v4u{pk(d[0], d[1]), tag, pk(d[2], d[3]), tag}, rws, WS_FF + u * 5120 + w * 512 + (g >> 1) * 2560 + n * 32 + (g & 1) * 16);
      }
      ST(11);
    }
    if (u >= 88) {
      const int T = u - 88;
      {
        v4u f[2];
        const int p = tid >> 2;
        const int vo = (p < 108 ? p : 0) * 5120 + (tid & 3) * 16, so = WS_FF + T * 64;
        for (u32 it = 0; it < SPIN_CAP; ++it) {
          POLL();
          f[0] = ldc(rws, w * 13 * 5120, so);
          if (!anyset(tagbad(f[0], tag))) break;
        }
        for (u32 it = 0; it < SPIN_CAP; ++it) {
          POLL();
          f[0] = ldc(rws, vo, so);
          f[1] = ldc(rws, vo + 2560, so);
          if (!anyset(tagbad(f[0], tag) | tagbad(f[1], tag))) break;
        }
#pragma unroll
        for (int j = 0; j < 2; ++j) {
          f4 v = unpk(f[j]);
          if (p >= 108) v = f4{0.f, 0.f, 0.f, 0.f};
#pragma unroll
          for (int k = 0; k < 4; ++k) {
            float s = v[k];
            s += dpp<ROR4>(s);
            s += dpp<ROR8>(s);
            v[k] = xrow_sum(s);
          }
          if (lane < 4) *(LAS f4*)(L + L2_FR + w * 128 + j * 64 + lane * 16) = v;
        }
      }
      ST(12);
      {
        v4u a0, a1;
        const int so = WS_FA + T * 16384;
        for (u32 it = 0; it < SPIN_CAP; ++it) {
          POLL();
          a0 = ldc(rws, w * 1024, so);
          if (!anyset(tagbad(a0, tag))) break;
        }
        asm volatile("" ::: "memory");
        a0 = ldc(rws, tid * 16, so);
        a1 = ldc(rws, tid * 16 + 8192, so);
        for (u32 it = 0; it < SPIN_CAP; ++it) {
          const bool b0 = anyset(tagbad(a0, tag)), b1 = anyset(tagbad(a1, tag));
          if (!(b0 || b1)) break;
          POLL();
          if (b0) a0 = ldc(rws, tid * 16, so);
          if (b1) a1 = ldc(rws, tid * 16 + 8192, so);
        }
        ST(13);
        f4 v = unpk(a0) + unpk(a1);
#pragma unroll
        for (int k = 0; k < 4; ++k) {
          float s = v[k];
          s += dpp<ROR8>(s);
          v[k] = xrow_sum(s);
        }
        if (lane < 8) *(LAS f4*)(L + L2_AR + w * 128 + lane * 16) = v;
      }
      bar();
      if (w == 0 && !(lane & 16)) {
        const int bb = lane >> 5;
        float A = 0.f, F = 0.f;
#pragma unroll
        for (int k = 0; k < 8; ++k) {
          A += *(LAS float*)(L + L2_AR + k * 128 + n * 8 + bb * 4);
          F += *(LAS float*)(L + L2_FR + k * 128 + bb * 64 + n * 4);
        }
        const float o = gate * A + (1.f - gate) * F;
        out[bb * 640 + T * 16 + n] = (u16)pk(o, o);
        if (T == 0 && lane == 0) stc1(tag, rws, 0, WS_EPOCH);
      }
      ST(14);
    }
  }
  asm volatile("" ::"v"(pf_));
}

extern "C" void kernel_launch(void* const* d_in, const int* in_sizes, int n_in, void* d_out, int out_size, void* d_ws, size_t ws_size,
                              hipStream_t stream) {
  mla_ffn_mix<<<256, 512, 0, stream>>>((const u16*)d_in[0], (const u16*)d_in[1], (const u16*)d_in[2], (const u16*)d_in[3],
                                       (const u16*)d_in[4], (const u16*)d_in[5], (const u16*)d_in[7], (const u16*)d_in[8],
                                       (const u16*)d_in[9], (const u16*)d_in[10], (u16*)d_out, (unsigned char*)d_ws);
}
```

```cpp
#define STAMPS
#define PROBE_BID 0
#define PROBE_A 0
#define PROBE_B 3
#include <hip/hip_runtime.h>
#include <stdint.h>

typedef unsigned u32;
typedef unsigned long long u64;
typedef unsigned short u16;
typedef u32 v4u __attribute__((ext_vector_type(4)));
typedef u32 v3u __attribute__((ext_vector_type(3)));
typedef u32 v2u __attribute__((ext_vector_type(2)));
typedef float f4 __attribute__((ext_vector_type(4)));
typedef __bf16 bf8 __attribute__((ext_vector_type(8)));
typedef __bf16 bf2 __attribute__((ext_vector_type(2)));
typedef float f2 __attribute__((ext_vector_type(2)));
typedef short s4 __attribute__((ext_vector_type(4)));
#define GAS __attribute__((address_space(1)))
#define LAS __attribute__((address_space(3)))
typedef __amdgpu_buffer_rsrc_t rsrc_t;

constexpr int AUX_NT = 2, AUX_SC1 = 16;
#define WPOL AUX_NT
#define CODE_BYTES 15360
constexpr u32 SPIN_CAP = 1u << 17;
constexpr u32 SENT = 0xFFFFFFFFu;

constexpr int WS_EPOCH = 0;
constexpr int WS_ML = 1024;
constexpr int WS_QP = 65536;
constexpr int WS_QA = WS_QP + 256 * 1024;
constexpr int WS_FF = WS_QA + 128 * 1024;
constexpr int WS_FA = WS_FF + 108 * 5120;
constexpr int WS_OB = 2 * 1024 * 1024;
constexpr int WS_END = WS_OB + 2 * 1024 * 1024;
static_assert(WS_FA % 128 == 0 && WS_FA + 40 * 128 * 128 <= WS_OB, "");

constexpr int RS = 1056;
constexpr int L2_QF = 128 * RS;
constexpr int L2_PF = L2_QF + 16 * RS;
constexpr int L2_MX = L2_PF + 4096;
constexpr int L2_LS = L2_MX + 512;
constexpr int L2_FR = L2_LS + 512;
constexpr int L2_AR = L2_FR + 1024;
constexpr int L2_H = L2_AR + 1024;
constexpr int LDS_BYTES = L2_H + 64;
constexpr int L1_UV = 0, L1_WO = 32768, L1_UK = L1_WO, L1_RED = L1_WO + 81920, L1_LA = L1_RED + 8192, L1_CX = L1_LA + 2048, L1_Q = L1_CX + 8 * 144, L1_W = L1_Q + 576;
static_assert(L1_Q + 1024 <= LDS_BYTES && LDS_BYTES <= 163840, "");

__device__ __forceinline__ f4 mfma(v4u a, v4u b, f4 c) {
  return __builtin_amdgcn_mfma_f32_16x16x32_bf16(__builtin_bit_cast(bf8, a), __builtin_bit_cast(bf8, b), c, 0, 0, 0);
}
__device__ __forceinline__ u32 pk(float lo, float hi) {
  f2 v = {lo, hi};
  return __builtin_bit_cast(u32, __builtin_convertvector(v, bf2));
}
__device__ __forceinline__ float blo(u32 v) { return __uint_as_float(v << 16); }
__device__ __forceinline__ float bhi(u32 v) { return __uint_as_float(v & 0xFFFF0000u); }
template <int CTRL> __device__ __forceinline__ float dpp(float x) {
  return __builtin_bit_cast(float, __builtin_amdgcn_mov_dpp(__builtin_bit_cast(int, x), CTRL, 0xf, 0xf, true));
}
constexpr int XOR1 = 0xB1, XOR2 = 0x4E, ROR8 = 0x128, ROR4 = 0x124;
__device__ __forceinline__ float xrow_max(float x) {
  auto s = __builtin_amdgcn_permlane16_swap(__float_as_uint(x), __float_as_uint(x), false, false);
  x = fmaxf(__uint_as_float(s[0]), __uint_as_float(s[1]));
  auto t = __builtin_amdgcn_permlane32_swap(__float_as_uint(x), __float_as_uint(x), false, false);
  return fmaxf(__uint_as_float(t[0]), __uint_as_float(t[1]));
}
__device__ __forceinline__ float xrow_sum(float x) {
  auto s = __builtin_amdgcn_permlane16_swap(__float_as_uint(x), __float_as_uint(x), false, false);
  x = __uint_as_float(s[0]) + __uint_as_float(s[1]);
  auto t = __builtin_amdgcn_permlane32_swap(__float_as_uint(x), __float_as_uint(x), false, false);
  return __uint_as_float(t[0]) + __uint_as_float(t[1]);
}
__device__ __forceinline__ rsrc_t mk(const void* p, u32 bytes) { return __builtin_amdgcn_make_buffer_rsrc((void*)p, 0, bytes, 0x00020000); }
__device__ __forceinline__ v4u ldw(rsrc_t r, int vo, int so) { return __builtin_amdgcn_raw_buffer_load_b128(r, vo, so, WPOL); }
__device__ __forceinline__ v4u ldc(rsrc_t r, int vo, int so) { return __builtin_amdgcn_raw_buffer_load_b128(r, vo, so, AUX_SC1); }
__device__ __forceinline__ void stc(v4u v, rsrc_t r, int vo) { __builtin_amdgcn_raw_buffer_store_b128(v, r, vo, 0, AUX_SC1); }
__device__ __forceinline__ void stc2(v2u v, rsrc_t r, int vo, int so) { __builtin_amdgcn_raw_buffer_store_b64(v, r, vo, so, AUX_SC1); }
__device__ __forceinline__ void stc1(u32 v, rsrc_t r, int vo, int so) { __builtin_amdgcn_raw_buffer_store_b32(v, r, vo, so, AUX_SC1); }
__device__ __forceinline__ void dma(rsrc_t r, LAS unsigned char* l, int vo, int so) {
  __builtin_amdgcn_raw_ptr_buffer_load_lds(r, (LAS void*)l, 16, vo, so, 0, WPOL);
}
__device__ __forceinline__ v2u trd(LAS unsigned char* p) {
  return __builtin_bit_cast(v2u, __builtin_amdgcn_ds_read_tr16_b64_v4i16((LAS s4*)p));
}
__device__ __forceinline__ void bar() {
  asm volatile("s_waitcnt lgkmcnt(0)" ::: "memory");
  __builtin_amdgcn_s_barrier();
  asm volatile("" ::: "memory");
}
#define PSLEEP 0
#define POLL() do { if (PSLEEP) __builtin_amdgcn_s_sleep(PSLEEP); asm volatile("" ::: "memory"); } while (0)
#define G1_DELAY 0
#define G2_DELAY 0
#define FFN_POS 0
__device__ __forceinline__ void delay(int ticks) {
  if (ticks) {
    const u64 t0 = __builtin_amdgcn_s_memrealtime();
    while (__builtin_amdgcn_s_memrealtime() < t0 + ticks) __builtin_amdgcn_s_sleep(1);
  }
}
#define DRAIN() asm volatile("s_waitcnt vmcnt(0)" ::: "memory")
#define SCHED() __builtin_amdgcn_sched_barrier(0)
__device__ __forceinline__ bool anyset(u32 bad) { return __builtin_amdgcn_ballot_w64(bad != 0u) != 0ull; }
__device__ __forceinline__ u32 tagbad(v4u a, u32 tag) { return (a.y ^ tag) | (a.w ^ tag); }
__device__ __forceinline__ bool sentbad(v4u a) { return __builtin_amdgcn_ballot_w64(max(max(a.x, a.y), max(a.z, a.w)) == SENT) != 0ull; }
__device__ __forceinline__ v4u selp(u32 v, int n) {
  const int q = (n >> 1) & 3;
  return v4u{q == 0 ? v : 0u, q == 1 ? v : 0u, q == 2 ? v : 0u, q == 3 ? v : 0u};
}
__device__ __forceinline__ u32 plo(u32 d, int n) { return __builtin_amdgcn_perm(0u, d, (n & 1) ? 0x01000c0cu : 0x0c0c0100u); }
__device__ __forceinline__ u32 phi(u32 d, int n) { return __builtin_amdgcn_perm(0u, d, (n & 1) ? 0x03020c0cu : 0x0c0c0302u); }
__device__ __forceinline__ f4 gemv5(v3u xd, const v4u (&wv)[5], int n, int g) {
  const u32 sh = (g & 1) << 4;
  const u32 a0 = __builtin_amdgcn_alignbit(xd.y, xd.x, sh), a1 = __builtin_amdgcn_alignbit(xd.z, xd.y, sh), a2 = xd.z >> sh;
  const v4u s0 = selp(plo(a0, n), n), s1 = selp(phi(a0, n), n), s2 = selp(plo(a1, n), n), s3 = selp(phi(a1, n), n), s4_ = selp(plo(a2, n), n);
  f4 acc = {0.f, 0.f, 0.f, 0.f};
  acc = mfma(s0, wv[0], acc);
  acc = mfma(s1, wv[1], acc);
  acc = mfma(s2, wv[2], acc);
  acc = mfma(s3, wv[3], acc);
  acc = mfma(s4_, wv[4], acc);
  return acc;
}
__device__ __forceinline__ f4 unpk(v4u a) { return f4{blo(a.x), bhi(a.x), blo(a.z), bhi(a.z)}; }

#define NST 16
#define ST(i) do { SCHED(); asm volatile("s_memrealtime %0\n s_waitcnt lgkmcnt(0)" : "=s"(st_[i])); SCHED(); } while (0)

extern "C" __global__ void __launch_bounds__(512, 2)
mla_ffn_mix(const u16* __restrict__ x, const u16* __restrict__ ckv, const u16* __restrict__ wq, const u16* __restrict__ wuk,
            const u16* __restrict__ wuv, const u16* __restrict__ wo, const u16* __restrict__ wg, const u16* __restrict__ wu,
            const u16* __restrict__ wd, const u16* __restrict__ gw, u16* __restrict__ out, unsigned char* __restrict__ ws) {
  __shared__ __attribute__((aligned(16))) unsigned char smem[LDS_BYTES];
  LAS unsigned char* L = (LAS unsigned char*)smem;
  u64 st_[NST];
#pragma unroll
  for (int i = 0; i < NST; ++i) st_[i] = 0;
  ST(0);
  const int tid = threadIdx.x, lane = tid & 63, w = __builtin_amdgcn_readfirstlane(tid >> 6);
  const int bid = blockIdx.x, xcd = bid & 7, r = bid >> 3;
  const int n = lane & 15, g = lane >> 4;
  u32 pf_ = 0;
  if (w < 2) {
    u64 pc;
    asm volatile("s_getpc_b64 %0" : "=s"(pc));
    const GAS unsigned char* cb = (const GAS unsigned char*)(pc & ~255ull);
    pf_ = *(const GAS u32*)(cb + min(tid * 128, CODE_BYTES - 128));
  }
  asm volatile("" ::"s"(x), "s"(ckv), "s"(wq), "s"(wuk), "s"(wuv), "s"(wo), "s"(wg), "s"(wu), "s"(wd), "s"(gw), "s"(out), "s"(ws));
  const rsrc_t rws = mk(ws, WS_END);

  if (__builtin_expect(r < 16, 1)) {
    const int h = 4 * xcd + (r >> 2), i = r & 3;
    v4u wv[10];
    u32 tag;
    if (w < 4) {
      const rsrc_t rx = mk(x, 2560), rq = mk(wq, 640 * 8192);
      const int row = 160 * i + 40 * w + 10 * g;
      const v4u xa = __builtin_amdgcn_raw_buffer_load_b128(rx, ((n >> 3) * 640 + row) * 2, 0, 0);
      const u32 xb = __builtin_amdgcn_raw_buffer_load_b32(rx, ((n >> 3) * 640 + row) * 2 + 16, 0, 0);
#pragma unroll
      for (int t = 0; t < 10; ++t) wv[t] = ldw(rq, row * 8192 + h * 256 + n * 16, t * 8192);
      tag = __builtin_amdgcn_raw_buffer_load_b32(rws, 0, WS_EPOCH, 0);
      SCHED();
      ST(1);
      f4 acc = {0.f, 0.f, 0.f, 0.f};
#pragma unroll
      for (int t = 0; t < 5; ++t) {
        const u32 xx = t < 4 ? xa[t & 3] : xb;
        acc = mfma(selp(plo(xx, n), n), wv[2 * t], acc);
        acc = mfma(selp(phi(xx, n), n), wv[2 * t + 1], acc);
      }
      *(LAS f4*)(L + L1_RED + w * 1024 + lane * 16) = acc;
      ST(2);
      bar();
      tag += 1u;
      if (w == 0) {
        f4 s = (acc + *(LAS f4*)(L + L1_RED + 1024 + lane * 16)) + (*(LAS f4*)(L + L1_RED + 2048 + lane * 16) + *(LAS f4*)(L + L1_RED + 3072 + lane * 16));
        stc(v4u{pk(s[0], s[1]), tag, pk(s[2], s[3]), tag}, rws, WS_QP + (h * 4 + i) * 1024 + lane * 16);
        ST(3);
        v4u q1, q2, q3;
        for (u32 it = 0; it < SPIN_CAP; ++it) {
          POLL();
          q1 = ldc(rws, lane * 16, WS_QP + (h * 4 + (i ^ 1)) * 1024);
          q2 = ldc(rws, lane * 16, WS_QP + (h * 4 + (i ^ 2)) * 1024);
          q3 = ldc(rws, lane * 16, WS_QP + (h * 4 + (i ^ 3)) * 1024);
          if (!anyset(tagbad(q1, tag) | tagbad(q2, tag) | tagbad(q3, tag))) break;
        }
        s = (s + unpk(q1)) + (unpk(q2) + unpk(q3));
        *(LAS v2u*)(L + L1_Q + (g >> 1) * 272 + n * 16 + (g & 1) * 8) = v2u{pk(s[0], s[1]), pk(s[2], s[3])};
        ST(4);
      }
      bar();
    } else {
      const rsrc_t rk = mk(wuk, 512 * 8192);
      tag = __builtin_amdgcn_raw_buffer_load_b32(rws, 0, WS_EPOCH, 0);
#pragma unroll
      for (int k = 0; k < 8; ++k) wv[k] = ldw(rk, g * 8192 + n * 16, (128 * i + 32 * (w - 4) + 4 * k) * 8192 + h * 256);
      SCHED();
      ST(1);
      __builtin_amdgcn_s_barrier();
      tag += 1u;
      LAS unsigned char* U = L + L1_UK + (w - 4) * 8704;
#pragma unroll
      for (int k = 0; k < 8; ++k) *(LAS v4u*)(U + (4 * k + g) * 272 + n * 16) = wv[k];
#pragma unroll
      for (int k = 0; k < 8; ++k) wv[k] = *(LAS v4u*)(U + (2 * n + (k >> 2)) * 272 + (k & 3) * 64 + g * 16);
      ST(2);
      bar();
      f4 a0 = {0.f, 0.f, 0.f, 0.f}, a1 = {0.f, 0.f, 0.f, 0.f};
#pragma unroll
      for (int s = 0; s < 4; ++s) {
        const v4u qf = *(LAS v4u*)(L + L1_Q + (n & 1) * 272 + s * 64 + g * 16);
        a0 = mfma(qf, wv[s], a0);
        a1 = mfma(qf, wv[4 + s], a1);
      }
      constexpr float SC = 0.08838834764831845f * 1.4426950408889634f;
      const float v0 = ((g & 1) ? a0[1] : a0[0]) * SC, v1 = ((g & 1) ? a1[1] : a1[0]) * SC;
      if (lane < 32) stc2(v2u{pk(v0, v1), tag}, rws, g * 65536 + n * 8, WS_QA + h * 2048 + i * 512 + (w - 4) * 128);
    }
    SCHED();
    __builtin_amdgcn_s_barrier();
    ST(5);
    delay(G1_DELAY);
    const int dh = i >> 1, ch = i & 1;
    {
      const rsrc_t rv = mk(wuv, 512 * 8192), ro = mk(wo, 4096 * 1280);
      const int lr = lane >> 3;
#pragma unroll
      for (int k = 0; k < 4; ++k)
        dma(rv, L + L1_UV + (4 * w + k) * 1024, lr * 8192 + (((lane & 7) ^ (((2 * w + (k >> 1)) & 3) << 1)) << 4),
            (256 * ch + 32 * w + 8 * k) * 8192 + h * 256 + dh * 128);
      const int ck = (lane & 7) ^ (((lr >> 1) & 3) << 1);
#pragma unroll
      for (int k = 0; k < 10; ++k) {
        const int blk = k + 5 * ch >= 10 ? k + 5 * ch - 10 : k + 5 * ch;
        dma(ro, L + L1_WO + blk * 8192 + w * 1024, lr * 1280 + ck * 16, (h * 128 + 64 * dh + 8 * w) * 1280 + blk * 128);
      }
      SCHED();
    }
    ST(6);
    const int b = w >> 2, cb = w & 3;
    v4u ov[4];
    v2u fv[16], fo[20];
    {
      DRAIN();
      ST(7);
      bar();
      {
        const int q = n >> 2, p = n & 3;
        const int rbv = (16 * q + g) * 128 + p * 8;
#pragma unroll
        for (int t = 0; t < 4; ++t)
#pragma unroll
          for (int e = 0; e < 4; ++e) fv[t * 4 + e] = trd(L + L1_UV + cb * 8192 + ((e & 1) * 8 + (e >> 1) * 4) * 128 + rbv + ((t ^ q) << 5));
        const int sw = 2 * (g & 1) + (q >> 1), rb = (4 * g + q) * 128 + p * 8;
#pragma unroll
        for (int t = 0; t < 5; ++t) {
          const int T = 5 * w + t;
#pragma unroll
          for (int e = 0; e < 4; ++e) fo[t * 4 + e] = trd(L + L1_WO + (T >> 2) * 8192 + e * 2048 + rb + (((T & 3) ^ sw) << 5));
        }
      }
      v4u ml;
      if (w == 0) {
        for (u32 it = 0; it < SPIN_CAP; ++it) {
          POLL();
          ml = ldc(rws, lane * 512, WS_ML + h * 16);
          if (!anyset(tagbad(ml, tag))) break;
        }
        ST(11);
      }
      SCHED();
      __builtin_amdgcn_s_barrier();
      asm volatile("" ::: "memory");
      const int kg0 = 8 * g + 4 * (n >> 3);
      const int vo = kg0 * 1024 + (n & 7) * 16, so = WS_OB + (b * 32 + h) * 32768 + ch * 512 + cb * 128;
#pragma unroll
      for (int t = 0; t < 4; ++t) ov[t] = ldc(rws, vo + t * 1024, so);
      SCHED();
      if (w == 0) {
        const float m = __uint_as_float(ml.x);
        float M = fmaxf(m, dpp<XOR1>(m));
        M = fmaxf(M, dpp<XOR2>(M));
        M = fmaxf(M, dpp<ROR4>(M));
        M = fmaxf(M, dpp<ROR8>(M));
        auto sm = __builtin_amdgcn_permlane16_swap(__float_as_uint(M), __float_as_uint(M), false, false);
        M = fmaxf(__uint_as_float(sm[0]), __uint_as_float(sm[1]));
        const float e = __builtin_amdgcn_exp2f(m - M);
        const u32 wb = pk(e, e);
        *(LAS u16*)(L + L1_W + lane * 2) = (u16)wb;
        float Ls = blo(wb) * __uint_as_float(ml.z);
        Ls += dpp<XOR1>(Ls);
        Ls += dpp<XOR2>(Ls);
        Ls += dpp<ROR4>(Ls);
        Ls += dpp<ROR8>(Ls);
        auto sl = __builtin_amdgcn_permlane16_swap(__float_as_uint(Ls), __float_as_uint(Ls), false, false);
        *(LAS float*)(L + L1_W + 128 + (lane >> 5) * 4) = __builtin_amdgcn_rcpf(__uint_as_float(sl[0]) + __uint_as_float(sl[1]));
      }
      bar();
      const v2u w4 = *(LAS v2u*)(L + L1_W + b * 64 + kg0 * 2);
      const float inv = *(LAS float*)(L + L1_W + 128 + b * 4);
      const v4u aw0 = selp(plo(w4.x, n), n), aw1 = selp(phi(w4.x, n), n), aw2 = selp(plo(w4.y, n), n), aw3 = selp(phi(w4.y, n), n);
      SCHED();
      for (u32 it = 0; it < SPIN_CAP; ++it) {
        const bool b0 = sentbad(ov[0]), b1 = sentbad(ov[1]), b2 = sentbad(ov[2]), b3 = sentbad(ov[3]);
        if (!(b0 || b1 || b2 || b3)) break;
        POLL();
        if (b0) ov[0] = ldc(rws, vo, so);
        if (b1) ov[1] = ldc(rws, vo + 1024, so);
        if (b2) ov[2] = ldc(rws, vo + 2048, so);
        if (b3) ov[3] = ldc(rws, vo + 3072, so);
      }
      ST(8);
      f4 acc = {0.f, 0.f, 0.f, 0.f};
      acc = mfma(aw0, ov[0], acc);
      acc = mfma(aw1, ov[1], acc);
      acc = mfma(aw2, ov[2], acc);
      acc = mfma(aw3, ov[3], acc);
      if ((g >> 1) == (n >> 3))
        *(LAS v2u*)(L + L1_LA + w * 256 + (g >> 1) * 128 + (n & 7) * 16 + (g & 1) * 8) = v2u{pk(acc[0] * inv, acc[1] * inv), pk(acc[2] * inv, acc[3] * inv)};
    }
    {
      const v4u a0 = *(LAS v4u*)(L + L1_LA + w * 256 + (n & 1) * 128 + g * 16), a1 = *(LAS v4u*)(L + L1_LA + w * 256 + (n & 1) * 128 + 64 + g * 16);
      float cv[4];
#pragma unroll
      for (int t = 0; t < 4; ++t) {
        f4 d = {0.f, 0.f, 0.f, 0.f};
        d = mfma(a0, v4u{fv[t * 4].x, fv[t * 4].y, fv[t * 4 + 1].x, fv[t * 4 + 1].y}, d);
        d = mfma(a1, v4u{fv[t * 4 + 2].x, fv[t * 4 + 2].y, fv[t * 4 + 3].x, fv[t * 4 + 3].y}, d);
        cv[t] = d[0] + d[1];
      }
      const float v01 = (g & 1) ? cv[1] : cv[0], v23 = (g & 1) ? cv[3] : cv[2], v = (g & 2) ? v23 : v01;
      *(LAS u16*)(L + L1_CX + w * 144 + (32 * (g >> 1) + 8 * (n >> 2) + 4 * (g & 1) + (n & 3)) * 2) = (u16)pk(v, v);
    }
    bar();
    ST(9);
    {
      const v4u a0 = *(LAS v4u*)(L + L1_CX + (n & 7) * 144 + g * 16), a1 = *(LAS v4u*)(L + L1_CX + (n & 7) * 144 + 64 + g * 16);
      u32 cv[5];
#pragma unroll
      for (int t = 0; t < 5; ++t) {
        f4 d = {0.f, 0.f, 0.f, 0.f};
        d = mfma(a0, v4u{fo[t * 4].x, fo[t * 4].y, fo[t * 4 + 1].x, fo[t * 4 + 1].y}, d);
        d = mfma(a1, v4u{fo[t * 4 + 2].x, fo[t * 4 + 2].y, fo[t * 4 + 3].x, fo[t * 4 + 3].y}, d);
        const u32 sv = __float_as_uint((d[0] + d[1]) + (d[2] + d[3]));
        auto sp = __builtin_amdgcn_permlane16_swap(sv, sv, false, false);
        cv[t] = pk(__uint_as_float(sp[0]), __uint_as_float(sp[1]));
      }
      const int so = WS_FA + 5 * w * 16384 + (h * 4 + i) * 128;
      const u32 c01 = (g & 1) ? cv[1] : cv[0], c23 = (g & 1) ? cv[3] : cv[2];
      stc2(v2u{(g & 2) ? c23 : c01, tag}, rws, g * 16384 + n * 8, so);
      if (lane < 16) stc2(v2u{cv[4], tag}, rws, n * 8, so + 4 * 16384);
    }
    ST(10);
  } else {
    const int u = xcd * 16 + (r - 16), hg = r & 1, bk = u >> 1, b = bk >> 5, kg = bk & 31;
    u32 tag = __builtin_amdgcn_raw_buffer_load_b32(rws, 0, WS_EPOCH, 0);
    const int o_vo = WS_OB + (b * 32 + 16 * hg + 4 * g + 2 * (n >> 3)) * 32768 + kg * 1024 + w * 128 + (n & 7) * 16;
    stc(v4u{SENT, SENT, SENT, SENT}, rws, o_vo);
    stc(v4u{SENT, SENT, SENT, SENT}, rws, o_vo + 32768);
    delay(G2_DELAY);
    ST(1);
    {
      const rsrc_t rc = mk(ckv, 2 * 4096 * 1024);
#pragma unroll
      for (int k = 0; k < 16; ++k) {
        const int row = 16 * w + ((k + 8 * hg) & 15);
        dma(rc, L + row * RS, lane * 16, (bk * 128 + row) * 1024);
      }
      SCHED();
    }
    ST(2);
    const int fs = u >> 2, mq = u & 3;
    const int frow = 160 * mq + 20 * w + 5 * g;
    float gate = 0.f;
    v3u xd;
    {
      const rsrc_t rx = mk(x, 2560), rg = mk(gw, 1280);
      xd = __builtin_amdgcn_raw_buffer_load_b96(rx, (((n >> 3) * 640 + frow) * 2) & ~3, 0, 0);
      if (w == 0 && u >= 88) {
        float s = 0.f;
#pragma unroll
        for (int k = 0; k < 5; ++k) {
          const v2u a = __builtin_amdgcn_raw_buffer_load_b64(rx, (lane >> 5) * 1280 + (lane & 31) * 8 + k * 256, 0, 0);
          const v2u c = __builtin_amdgcn_raw_buffer_load_b64(rg, (lane & 31) * 8 + k * 256, 0, 0);
          s += (blo(a.x) * blo(c.x) + bhi(a.x) * bhi(c.x)) + (blo(a.y) * blo(c.y) + bhi(a.y) * bhi(c.y));
        }
        s += dpp<XOR1>(s);
        s += dpp<XOR2>(s);
        s += dpp<ROR4>(s);
        s += dpp<ROR8>(s);
        auto sp = __builtin_amdgcn_permlane16_swap(__float_as_uint(s), __float_as_uint(s), false, false);
        s = __uint_as_float(sp[0]) + __uint_as_float(sp[1]);
        gate = 1.f / (1.f + __expf(-s));
      }
    }
    DRAIN();
    ST(3);
    tag += 1u;
    v4u kf[16];
#pragma unroll
    for (int s = 0; s < 16; ++s) kf[s] = *(LAS v4u*)(L + (16 * w + n) * RS + s * 64 + g * 16);
    bar();
    v2u vf[32];
    {
      v4u qa[4];
      const int so = WS_QA + (b * 32 + 16 * hg + 2 * w) * 2048;
#pragma unroll
      for (int k = 0; k < 4; ++k) qa[k] = ldc(rws, lane * 16 + k * 1024, so);
      SCHED();
      {
        const int rb = (4 * g + (n >> 2)) * RS + w * 128 + (n & 3) * 8;
#pragma unroll
        for (int t = 0; t < 4; ++t)
#pragma unroll
          for (int e = 0; e < 8; ++e) vf[t * 8 + e] = trd(L + rb + e * 16 * RS + t * 32);
      }
      SCHED();
      ST(4);
      for (u32 it = 0; it < SPIN_CAP; ++it) {
        const bool b0 = anyset(tagbad(qa[0], tag)), b1 = anyset(tagbad(qa[1], tag)), b2 = anyset(tagbad(qa[2], tag)), b3 = anyset(tagbad(qa[3], tag));
        if (!(b0 || b1 || b2 || b3)) break;
        POLL();
        if (b0) qa[0] = ldc(rws, lane * 16, so);
        if (b1) qa[1] = ldc(rws, lane * 16 + 1024, so);
        if (b2) qa[2] = ldc(rws, lane * 16 + 2048, so);
        if (b3) qa[3] = ldc(rws, lane * 16 + 3072, so);
      }
#pragma unroll
      for (int k = 0; k < 4; ++k) *(LAS v2u*)(L + L2_QF + (2 * w + (k >> 1)) * RS + (k & 1) * 512 + lane * 8) = v2u{qa[k].x, qa[k].z};
    }
    ST(5);
    bar();
    v4u wv[5], dv[4];
    f4 sa = {0.f, 0.f, 0.f, 0.f};
    {
      const GAS unsigned char* wp = (const GAS unsigned char*)(n < 8 ? wg : wu) + frow * 3456 + (fs < 27 ? fs : 0) * 128 + (n & 7) * 16;
      LAS unsigned char* Q = L + L2_QF + n * RS + g * 16;
      v4u qf[16];
#pragma unroll
      for (int s = 0; s < 8; ++s) qf[s] = *(LAS v4u*)(Q + s * 64);
      SCHED();
#pragma unroll
      for (int s = 0; s < 16; ++s) {
        sa = mfma(kf[s], qf[s], sa);
        if (s < 8) qf[s + 8] = *(LAS v4u*)(Q + (s + 8) * 64);
        if (FFN_POS == 0 && s % 3 == 0 && s < 15) wv[s / 3] = __builtin_nontemporal_load((const GAS v4u*)(wp + (s / 3) * 3456));
        SCHED();
      }
    }
    {
      const float m = xrow_max(fmaxf(fmaxf(sa[0], sa[1]), fmaxf(sa[2], sa[3])));
      if (g == 0) *(LAS float*)(L + L2_MX + n * 32 + w * 4) = m;
    }
    ST(6);
    bar();
    float M;
    {
      const f4 m0 = *(LAS f4*)(L + L2_MX + n * 32), m1 = *(LAS f4*)(L + L2_MX + n * 32 + 16);
      M = fmaxf(fmaxf(fmaxf(m0[0], m0[1]), fmaxf(m0[2], m0[3])), fmaxf(fmaxf(m1[0], m1[1]), fmaxf(m1[2], m1[3])));
      const u32 p01 = pk(__builtin_amdgcn_exp2f(sa[0] - M), __builtin_amdgcn_exp2f(sa[1] - M));
      const u32 p23 = pk(__builtin_amdgcn_exp2f(sa[2] - M), __builtin_amdgcn_exp2f(sa[3] - M));
      *(LAS v2u*)(L + L2_PF + (w >> 1) * 1024 + lane * 16 + (w & 1) * 8) = v2u{p01, p23};
      const float l = xrow_sum((blo(p01) + bhi(p01)) + (blo(p23) + bhi(p23)));
      if (g == 0) *(LAS float*)(L + L2_LS + n * 32 + w * 4) = l;
    }
    bar();
    ST(7);
    {
      v4u pf[4];
#pragma unroll
      for (int s = 0; s < 4; ++s) pf[s] = *(LAS v4u*)(L + L2_PF + s * 1024 + lane * 16);
      if (w == 0) {
        const f4 l0 = *(LAS f4*)(L + L2_LS + n * 32), l1 = *(LAS f4*)(L + L2_LS + n * 32 + 16);
        const float l = ((l0[0] + l0[1]) + (l0[2] + l0[3])) + ((l1[0] + l1[1]) + (l1[2] + l1[3]));
        if (lane < 16) stc(v4u{__float_as_uint(M), tag, __float_as_uint(l), tag}, rws, WS_ML + bk * 512 + (16 * hg + n) * 16);
      }
      f4 o[4];
#pragma unroll
      for (int t = 0; t < 4; ++t) o[t] = f4{0.f, 0.f, 0.f, 0.f};
#pragma unroll
      for (int s = 0; s < 4; ++s)
#pragma unroll
        for (int t = 0; t < 4; ++t)
          o[t] = mfma(pf[s], v4u{vf[t * 8 + 2 * s].x, vf[t * 8 + 2 * s].y, vf[t * 8 + 2 * s + 1].x, vf[t * 8 + 2 * s + 1].y}, o[t]);
      u32 pa[4], pb[4];
#pragma unroll
      for (int k = 0; k < 4; ++k) {
        pa[k] = min(pk(o[0][k], o[1][k]), SENT - 1u);
        pb[k] = min(pk(o[2][k], o[3][k]), SENT - 1u);
      }
#pragma unroll
      for (int k = 0; k < 2; ++k) {
        v4u pc;
        pc.x = __builtin_amdgcn_update_dpp(pa[k], pa[2 + k], ROR8, 0xf, 0xc, false);
        pc.y = __builtin_amdgcn_update_dpp(pb[k], pb[2 + k], ROR8, 0xf, 0xc, false);
        pc.z = __builtin_amdgcn_update_dpp(pa[2 + k], pa[k], ROR8, 0xf, 0x3, false);
        pc.w = __builtin_amdgcn_update_dpp(pb[2 + k], pb[k], ROR8, 0xf, 0x3, false);
        stc(pc, rws, o_vo + k * 32768);
      }
    }
    ST(8);
    if (u < 108) {
      SCHED();
      __builtin_amdgcn_s_barrier();
      if (FFN_POS == 1) {
        const GAS unsigned char* wp = (const GAS unsigned char*)(n < 8 ? wg : wu) + frow * 3456 + fs * 128 + (n & 7) * 16;
#pragma unroll
        for (int t = 0; t < 5; ++t) wv[t] = __builtin_nontemporal_load((const GAS v4u*)(wp + t * 3456));
        SCHED();
      }
      if (w < 5) {
        const rsrc_t rd = mk(wd, 1728 * 1280);
#pragma unroll
        for (int t = 0; t < 4; ++t) dv[t] = ldw(rd, g * 4 * 1280 + n * 16 + t * 1280, (64 * fs + 16 * mq) * 1280 + w * 256);
        SCHED();
      }
      *(LAS f4*)(L + L2_QF + w * 1024 + lane * 16) = gemv5(xd, wv, n, g);
      bar();
      if (w == 7) {
        f4 s = *(LAS f4*)(L + L2_QF + lane * 16);
#pragma unroll
        for (int k = 1; k < 8; ++k) s += *(LAS f4*)(L + L2_QF + k * 1024 + lane * 16);
        stc(v4u{pk(s[0], s[1]), tag, pk(s[2], s[3]), tag}, rws, WS_QP + (128 + u) * 1024 + lane * 16);
        ST(9);
        v4u q1, q2, q3;
        for (u32 it = 0; it < SPIN_CAP; ++it) {
          POLL();
          q1 = ldc(rws, lane * 16, WS_QP + (128 + (u ^ 1)) * 1024);
          q2 = ldc(rws, lane * 16, WS_QP + (128 + (u ^ 2)) * 1024);
          q3 = ldc(rws, lane * 16, WS_QP + (128 + (u ^ 3)) * 1024);
          if (!anyset(tagbad(q1, tag) | tagbad(q2, tag) | tagbad(q3, tag))) break;
        }
        s = (s + unpk(q1)) + (unpk(q2) + unpk(q3));
        float hv[4];
#pragma unroll
        for (int k = 0; k < 4; ++k) hv[k] = s[k] * dpp<ROR8>(s[k]) * __builtin_amdgcn_rcpf(1.f + __expf(-s[k]));
        if ((n >> 1) == mq) *(LAS v2u*)(L + L2_H + (g >> 1) * 32 + (n & 1) * 16 + (g & 1) * 8) = v2u{pk(hv[0], hv[1]), pk(hv[2], hv[3])};
        ST(10);
      }
      bar();
      if (w < 5) {
        const v2u hh = *(LAS v2u*)(L + L2_H + (n >> 3) * 32 + g * 8);
        f4 d = {0.f, 0.f, 0.f, 0.f};
        d = mfma(selp(plo(hh.x, n), n), dv[0], d);
        d = mfma(selp(phi(hh.x, n), n), dv[1], d);
        d = mfma(selp(plo(hh.y, n), n), dv[2], d);
        d = mfma(selp(phi(hh.y, n), n), dv[3], d);
        stc(v4u{pk(d[0], d[1]), tag, pk(d[2], d[3]), tag}, rws, WS_FF + u * 5120 + w * 512 + (g >> 1) * 2560 + n * 32 + (g & 1) * 16);
      }
      ST(11);
    }
    if (u >= 88) {
      const int T = u - 88;
      {
        v4u f[2];
        const int p = tid >> 2;
        const int vo = (p < 108 ? p : 0) * 5120 + (tid & 3) * 16, so = WS_FF + T * 64;
        for (u32 it = 0; it < SPIN_CAP; ++it) {
          POLL();
          f[0] = ldc(rws, w * 13 * 5120, so);
          if (!anyset(tagbad(f[0], tag))) break;
        }
        for (u32 it = 0; it < SPIN_CAP; ++it) {
          POLL();
          f[0] = ldc(rws, vo, so);
          f[1] = ldc(rws, vo + 2560, so);
          if (!anyset(tagbad(f[0], tag) | tagbad(f[1], tag))) break;
        }
#pragma unroll
        for (int j = 0; j < 2; ++j) {
          f4 v = unpk(f[j]);
          if (p >= 108) v = f4{0.f, 0.f, 0.f, 0.f};
#pragma unroll
          for (int k = 0; k < 4; ++k) {
            float s = v[k];
            s += dpp<ROR4>(s);
            s += dpp<ROR8>(s);
            v[k] = xrow_sum(s);
          }
          if (lane < 4) *(LAS f4*)(L + L2_FR + w * 128 + j * 64 + lane * 16) = v;
        }
      }
      ST(12);
      {
        v4u a0, a1;
        const int so = WS_FA + T * 16384;
        for (u32 it = 0; it < SPIN_CAP; ++it) {
          POLL();
          a0 = ldc(rws, w * 1024, so);
          if (!anyset(tagbad(a0, tag))) break;
        }
        asm volatile("" ::: "memory");
        a0 = ldc(rws, tid * 16, so);
        a1 = ldc(rws, tid * 16 + 8192, so);
        for (u32 it = 0; it < SPIN_CAP; ++it) {
          const bool b0 = anyset(tagbad(a0, tag)), b1 = anyset(tagbad(a1, tag));
          if (!(b0 || b1)) break;
          POLL();
          if (b0) a0 = ldc(rws, tid * 16, so);
          if (b1) a1 = ldc(rws, tid * 16 + 8192, so);
        }
        ST(13);
        f4 v = unpk(a0) + unpk(a1);
#pragma unroll
        for (int k = 0; k < 4; ++k) {
          float s = v[k];
          s += dpp<ROR8>(s);
          v[k] = xrow_sum(s);
        }
        if (lane < 8) *(LAS f4*)(L + L2_AR + w * 128 + lane * 16) = v;
      }
      bar();
      if (w == 0 && !(lane & 16)) {
        const int bb = lane >> 5;
        float A = 0.f, F = 0.f;
#pragma unroll
        for (int k = 0; k < 8; ++k) {
          A += *(LAS float*)(L + L2_AR + k * 128 + n * 8 + bb * 4);
          F += *(LAS float*)(L + L2_FR + k * 128 + bb * 64 + n * 4);
        }
        const float o = gate * A + (1.f - gate) * F;
        out[bb * 640 + T * 16 + n] = (u16)pk(o, o);
        if (T == 0 && lane == 0) stc1(tag, rws, 0, WS_EPOCH);
      }
      ST(14);
    }
  }
  asm volatile("" ::"v"(pf_));
  if (bid == PROBE_BID && w == 0) {
    const u64 e0 = __builtin_amdgcn_s_memrealtime();
    const u64 d = (st_[PROBE_B] - st_[PROBE_A]) * 10ull;
    while (__builtin_amdgcn_s_memrealtime() < e0 + (d < 100000ull ? d : 100000ull)) __builtin_amdgcn_s_sleep(2);
  }
}

extern "C" void kernel_launch(void* const* d_in, const int* in_sizes, int n_in, void* d_out, int out_size, void* d_ws, size_t ws_size,
                              hipStream_t stream) {
  mla_ffn_mix<<<256, 512, 0, stream>>>((const u16*)d_in[0], (const u16*)d_in[1], (const u16*)d_in[2], (const u16*)d_in[3],
                                       (const u16*)d_in[4], (const u16*)d_in[5], (const u16*)d_in[7], (const u16*)d_in[8],
                                       (const u16*)d_in[9], (const u16*)d_in[10], (u16*)d_out, (unsigned char*)d_ws);
}
```

```cpp
#include <hip/hip_runtime.h>
#include <stdint.h>

typedef unsigned u32;
typedef unsigned long long u64;
typedef unsigned short u16;
typedef u32 v4u __attribute__((ext_vector_type(4)));
typedef u32 v3u __attribute__((ext_vector_type(3)));
typedef u32 v2u __attribute__((ext_vector_type(2)));
typedef float f4 __attribute__((ext_vector_type(4)));
typedef __bf16 bf8 __attribute__((ext_vector_type(8)));
typedef __bf16 bf2 __attribute__((ext_vector_type(2)));
typedef float f2 __attribute__((ext_vector_type(2)));
typedef short s4 __attribute__((ext_vector_type(4)));
#define GAS __attribute__((address_space(1)))
#define LAS __attribute__((address_space(3)))
typedef __amdgpu_buffer_rsrc_t rsrc_t;

constexpr int AUX_NT = 2, AUX_SC1 = 16;
#define WPOL AUX_NT
#define CODE_BYTES 12416
#define G2_DELAY 0
#define G1_DELAY 0
#define HELP_UK 1
#define XSLEEP 5
#define FA_REP 1
constexpr u32 SPIN_CAP = 1u << 17;
constexpr u32 SENT = 0xFFFFFFFFu;
constexpr int OOB = 0x40000000;

constexpr int WS_EPOCH = 0;
constexpr int WS_ML = 1024;
constexpr int WS_QP = 65536;
constexpr int WS_QA = WS_QP + 640 * 1024;
constexpr int WS_FF = WS_QA + 128 * 1024;
constexpr int WS_FA = WS_FF + 108 * 5120;
constexpr int WS_OB = 2 * 1024 * 1024;
constexpr int WS_END = WS_OB + 2 * 1024 * 1024;
static_assert(WS_FA % 128 == 0 && WS_FA + 40 * 128 * 128 <= WS_OB, "");

constexpr int RS = 1056;
constexpr int L2_QF = 128 * RS;
constexpr int L2_PF = L2_QF + 16 * RS;
constexpr int L2_MX = L2_PF + 4096;
constexpr int L2_LS = L2_MX + 512;
constexpr int L2_FR = L2_LS + 512;
constexpr int L2_H = L2_FR + 1024;
constexpr int L_DUMP = L2_H + 512;
constexpr int LDS_BYTES = L_DUMP + 256;
constexpr int L1_UV = 0, L1_WO = 32768, L1_UK = L1_WO, L1_RED = L1_WO + 81920, L1_LA = L1_RED + 4096, L1_CX = L1_LA + 4096,
              L1_Q = L1_CX + 8 * 144, L1_W = L1_Q + 8 * 272;
static_assert(L1_W + 256 <= L_DUMP && LDS_BYTES <= 163840, "");

__device__ __forceinline__ f4 mfma(v4u a, v4u b, f4 c) {
  return __builtin_amdgcn_mfma_f32_16x16x32_bf16(__builtin_bit_cast(bf8, a), __builtin_bit_cast(bf8, b), c, 0, 0, 0);
}
__device__ __forceinline__ u32 pk(float lo, float hi) {
  f2 v = {lo, hi};
  return __builtin_bit_cast(u32, __builtin_convertvector(v, bf2));
}
__device__ __forceinline__ float blo(u32 v) { return __uint_as_float(v << 16); }
__device__ __forceinline__ float bhi(u32 v) { return __uint_as_float(v & 0xFFFF0000u); }
template <int CTRL> __device__ __forceinline__ float dpp(float x) {
  return __builtin_bit_cast(float, __builtin_amdgcn_mov_dpp(__builtin_bit_cast(int, x), CTRL, 0xf, 0xf, true));
}
constexpr int XOR1 = 0xB1, XOR2 = 0x4E, ROR8 = 0x128, ROR4 = 0x124;
__device__ __forceinline__ float x16_max(float x) {
  auto s = __builtin_amdgcn_permlane16_swap(__float_as_uint(x), __float_as_uint(x), false, false);
  return fmaxf(__uint_as_float(s[0]), __uint_as_float(s[1]));
}
__device__ __forceinline__ float x16_sum(float x) {
  auto s = __builtin_amdgcn_permlane16_swap(__float_as_uint(x), __float_as_uint(x), false, false);
  return __uint_as_float(s[0]) + __uint_as_float(s[1]);
}
__device__ __forceinline__ float x32_max(float x) {
  auto t = __builtin_amdgcn_permlane32_swap(__float_as_uint(x), __float_as_uint(x), false, false);
  return fmaxf(__uint_as_float(t[0]), __uint_as_float(t[1]));
}
__device__ __forceinline__ float x32_sum(float x) {
  auto t = __builtin_amdgcn_permlane32_swap(__float_as_uint(x), __float_as_uint(x), false, false);
  return __uint_as_float(t[0]) + __uint_as_float(t[1]);
}
__device__ __forceinline__ rsrc_t mk(const void* p, u32 bytes) { return __builtin_amdgcn_make_buffer_rsrc((void*)p, 0, bytes, 0x00020000); }
__device__ __forceinline__ v4u ldw(rsrc_t r, int vo, int so) { return __builtin_amdgcn_raw_buffer_load_b128(r, vo, so, WPOL); }
__device__ __forceinline__ v4u ldc(rsrc_t r, int vo, int so) { return __builtin_amdgcn_raw_buffer_load_b128(r, vo, so, AUX_SC1); }
__device__ __forceinline__ void stc(v4u v, rsrc_t r, int vo) { __builtin_amdgcn_raw_buffer_store_b128(v, r, vo, 0, AUX_SC1); }
__device__ __forceinline__ void stc2(v2u v, rsrc_t r, int vo, int so) { __builtin_amdgcn_raw_buffer_store_b64(v, r, vo, so, AUX_SC1); }
__device__ __forceinline__ void stc1(u32 v, rsrc_t r, int vo, int so) { __builtin_amdgcn_raw_buffer_store_b32(v, r, vo, so, AUX_SC1); }
__device__ __forceinline__ void dma(rsrc_t r, LAS unsigned char* l, int vo, int so) {
  __builtin_amdgcn_raw_ptr_buffer_load_lds(r, (LAS void*)l, 16, vo, so, 0, WPOL);
}
__device__ __forceinline__ void touch(rsrc_t r, LAS unsigned char* l, int vo, int so) {
  __builtin_amdgcn_raw_ptr_buffer_load_lds(r, (LAS void*)l, 4, vo, so, 0, WPOL);
}
__device__ __forceinline__ v2u trd(LAS unsigned char* p) {
  return __builtin_bit_cast(v2u, __builtin_amdgcn_ds_read_tr16_b64_v4i16((LAS s4*)p));
}
#define SCHED() __builtin_amdgcn_sched_barrier(0)
__device__ __forceinline__ void bar() {
  asm volatile("s_waitcnt lgkmcnt(0)" ::: "memory");
  __builtin_amdgcn_s_barrier();
  asm volatile("" ::: "memory");
}
__device__ __forceinline__ void bar0() {
  SCHED();
  asm volatile("" ::: "memory");
  __builtin_amdgcn_s_barrier();
  asm volatile("" ::: "memory");
  SCHED();
}
#define POLL() asm volatile("" ::: "memory")
#define DRAIN() asm volatile("s_waitcnt vmcnt(0)" ::: "memory")
__device__ __forceinline__ u64 tagbad(v4u a, u32 tag) { return __builtin_amdgcn_ballot_w64(a.y != tag) | __builtin_amdgcn_ballot_w64(a.w != tag); }
__device__ __forceinline__ u64 sentbad(v4u a) { return __builtin_amdgcn_ballot_w64(max(max(a.x, a.y), max(a.z, a.w)) == SENT); }
#define RL1(i) "s_mov_b64 exec, %[m" #i "]\n\ts_cbranch_execz " #i "f\n\tbuffer_load_dwordx4 %[a" #i "], %[v" #i "], %[rs], %[so] offen offset:%[o" #i "] sc1\n" #i ":\n\t"
template <int O1, int O2, int O3, int O4>
__device__ __forceinline__ void reload4(v4u& a1, v4u& a2, v4u& a3, v4u& a4, u64 m1, u64 m2, u64 m3, u64 m4, int v1, int v2, int v3, int v4,
                                        v4u rs, int so) {
  asm volatile("s_nop 4\n\t" RL1(1) RL1(2) RL1(3) RL1(4) "s_mov_b64 exec, -1\n\ts_waitcnt vmcnt(0)"
               : [a1] "+v"(a1), [a2] "+v"(a2), [a3] "+v"(a3), [a4] "+v"(a4)
               : [m1] "s"(m1), [m2] "s"(m2), [m3] "s"(m3), [m4] "s"(m4), [v1] "v"(v1), [v2] "v"(v2), [v3] "v"(v3), [v4] "v"(v4), [rs] "s"(rs),
                 [so] "s"(so), [o1] "i"(O1), [o2] "i"(O2), [o3] "i"(O3), [o4] "i"(O4)
               : "memory");
}
__device__ __forceinline__ void reload3(v4u& a1, v4u& a2, v4u& a3, u64 m1, u64 m2, u64 m3, int v1, int v2, int v3, v4u rs, int so) {
  asm volatile("s_nop 4\n\t" RL1(1) RL1(2) RL1(3) "s_mov_b64 exec, -1\n\ts_waitcnt vmcnt(0)"
               : [a1] "+v"(a1), [a2] "+v"(a2), [a3] "+v"(a3)
               : [m1] "s"(m1), [m2] "s"(m2), [m3] "s"(m3), [v1] "v"(v1), [v2] "v"(v2), [v3] "v"(v3), [rs] "s"(rs), [so] "s"(so), [o1] "i"(0),
                 [o2] "i"(0), [o3] "i"(0)
               : "memory");
}
__device__ __forceinline__ void reload1(v4u& a1, u64 m1, int v1, v4u rs, int so) {
  asm volatile("s_nop 4\n\t" RL1(1) "s_mov_b64 exec, -1\n\ts_waitcnt vmcnt(0)"
               : [a1] "+v"(a1)
               : [m1] "s"(m1), [v1] "v"(v1), [rs] "s"(rs), [so] "s"(so), [o1] "i"(0)
               : "memory");
}
__device__ __forceinline__ v4u selp(u32 v, int n) {
  const int q = (n >> 1) & 3;
  return v4u{q == 0 ? v : 0u, q == 1 ? v : 0u, q == 2 ? v : 0u, q == 3 ? v : 0u};
}
__device__ __forceinline__ u32 plo(u32 d, int n) { return __builtin_amdgcn_perm(0u, d, (n & 1) ? 0x01000c0cu : 0x0c0c0100u); }
__device__ __forceinline__ u32 phi(u32 d, int n) { return __builtin_amdgcn_perm(0u, d, (n & 1) ? 0x03020c0cu : 0x0c0c0302u); }
__device__ __forceinline__ f4 unpk(v4u a) { return f4{blo(a.x), bhi(a.x), blo(a.z), bhi(a.z)}; }
__device__ __forceinline__ f4 exchange4(f4 s, rsrc_t rws, v4u dws, int slot, int lane, u32 tag) {
  stc(v4u{pk(s[0], s[1]), tag, pk(s[2], s[3]), tag}, rws, WS_QP + slot * 1024 + lane * 16);
  const int v0 = (slot & 3) * 1024 + lane * 16, so = WS_QP + (slot & ~3) * 1024;
  DRAIN();
  if (XSLEEP) __builtin_amdgcn_s_sleep(XSLEEP);
  v4u q1 = ldc(rws, v0 ^ 1024, so), q2 = ldc(rws, v0 ^ 2048, so), q3 = ldc(rws, v0 ^ 3072, so);
_Pragma("unroll 1")
  for (u32 it = 0; it < SPIN_CAP; ++it) {
    const u64 m1 = tagbad(q1, tag), m2 = tagbad(q2, tag), m3 = tagbad(q3, tag);
    if (!(m1 | m2 | m3)) break;
    __builtin_amdgcn_s_sleep(2);
    reload3(q1, q2, q3, m1, m2, m3, v0 ^ 1024, v0 ^ 2048, v0 ^ 3072, dws, so);
  }
  return (s + unpk(q1)) + (unpk(q2) + unpk(q3));
}

#define ST(i)
#define STEND()

extern "C" __global__ void __launch_bounds__(512, 2)
mla_ffn_mix(const u16* __restrict__ x, const u16* __restrict__ ckv, const u16* __restrict__ wq, const u16* __restrict__ wuk,
            const u16* __restrict__ wuv, const u16* __restrict__ wo, const u16* __restrict__ wg, const u16* __restrict__ wu,
            const u16* __restrict__ wd, const u16* __restrict__ gw, u16* __restrict__ out, unsigned char* __restrict__ ws) {
  __shared__ __attribute__((aligned(16))) unsigned char smem[LDS_BYTES];
  LAS unsigned char* L = (LAS unsigned char*)smem;
  ST(0);
  const int tid = threadIdx.x, bid = blockIdx.x;
  {
    const u32 off = min((u32)(tid >> 2) * 128u, (u32)(CODE_BYTES - 128));
    u64 pc;
    asm volatile(
        "s_cmp_lt_u32 %2, 128\n\t"
        "s_cbranch_scc1 1f\n\t"
        "s_getpc_b64 %0\n\t"
        "s_andn2_b64 %0, %0, 0xff\n\t"
        "s_mov_b32 m0, %3\n\t"
        "s_nop 0\n\t"
        "global_load_lds_dword %1, %0\n"
        "1:"
        : "=&s"(pc)
        : "v"(off), "s"(bid), "s"((u32)(size_t)(L + L_DUMP))
        : "memory", "scc");
    SCHED();
  }
  const int lane = tid & 63, w = __builtin_amdgcn_readfirstlane(tid >> 6);
  const int xcd = bid & 7, r = bid >> 3;
  const int n = lane & 15, g = lane >> 4;
  asm volatile("" ::"s"(x), "s"(ckv), "s"(wq), "s"(wuk), "s"(wuv), "s"(wo), "s"(wg), "s"(wu), "s"(wd), "s"(gw), "s"(out), "s"(ws));
  const rsrc_t rws = mk(ws, WS_END);
  const v4u dws = {(u32)(size_t)ws, (u32)((size_t)ws >> 32) & 0xFFFFu, (u32)WS_END, 0x00020000u};

  if (__builtin_expect(r < 16, 1)) {
    const int h = 4 * xcd + (r >> 2), i = r & 3;
    u32 tag;
    if (__builtin_expect(w < 4, 1)) {
      v4u wv[10];
      const rsrc_t rx = mk(x, 2560), rq = mk(wq, 640 * 8192);
      const int row = 160 * i + 40 * w + 10 * g;
      const v4u xa = __builtin_amdgcn_raw_buffer_load_b128(rx, ((n >> 3) * 640 + row) * 2, 0, 0);
      const u32 xb = __builtin_amdgcn_raw_buffer_load_b32(rx, ((n >> 3) * 640 + row) * 2 + 16, 0, 0);
#pragma unroll
      for (int t = 0; t < 10; ++t) wv[t] = ldw(rq, row * 8192 + h * 256 + n * 16, t * 8192);
      tag = __builtin_amdgcn_raw_buffer_load_b32(rws, 0, WS_EPOCH, 0);
      bar0();
      __builtin_amdgcn_s_setprio(2);
      ST(1);
      v4u ax[10];
#pragma unroll
      for (int t = 0; t < 5; ++t) {
        const u32 xx = t < 4 ? xa[t & 3] : xb;
        ax[2 * t] = selp(plo(xx, n), n);
        ax[2 * t + 1] = selp(phi(xx, n), n);
      }
      SCHED();
      f4 acc = {0.f, 0.f, 0.f, 0.f};
#pragma unroll
      for (int t = 0; t < 10; ++t) acc = mfma(ax[t], wv[t], acc);
      ST(2);
      tag += 1u;
      acc = exchange4(acc, rws, dws, (h * 4 + w) * 4 + i, lane, tag);
      *(LAS v2u*)(L + L1_Q + (4 * (g >> 1) + w) * 272 + n * 16 + (g & 1) * 8) = v2u{pk(acc[0], acc[1]), pk(acc[2], acc[3])};
      ST(4);
      bar();
    } else {
      v4u wv[8];
      const rsrc_t rk = mk(wuk, 512 * 8192);
      bar0();
      tag = __builtin_amdgcn_raw_buffer_load_b32(rws, 0, WS_EPOCH, 0);
#pragma unroll
      for (int k = 0; k < 8; ++k) wv[k] = ldw(rk, g * 8192 + n * 16, (128 * i + 32 * (w - 4) + 4 * k) * 8192 + h * 256);
#pragma unroll
      for (int k = 0; k < 4; ++k)
        stc(v4u{SENT, SENT, SENT, SENT}, rws, WS_OB + (((w >> 1) & 1) * 32 + h) * 32768 + i * 8192 + (w & 1) * 4096 + k * 1024 + lane * 16);
      SCHED();
      ST(1);
      tag += 1u;
      LAS unsigned char* U = L + L1_UK + (w - 4) * 8704;
#pragma unroll
      for (int k = 0; k < 8; ++k) *(LAS v4u*)(U + (4 * k + g) * 272 + n * 16) = wv[k];
#pragma unroll
      for (int k = 0; k < 8; ++k) wv[k] = *(LAS v4u*)(U + (2 * n + (k >> 2)) * 272 + (k & 3) * 64 + g * 16);
      ST(2);
      DRAIN();
      ST(3);
      bar();
      f4 a0 = {0.f, 0.f, 0.f, 0.f}, a1 = {0.f, 0.f, 0.f, 0.f};
      v4u qf[4];
#pragma unroll
      for (int s = 0; s < 4; ++s) qf[s] = *(LAS v4u*)(L + L1_Q + (n & 7) * 272 + s * 64 + g * 16);
      SCHED();
#pragma unroll
      for (int s = 0; s < 4; ++s) {
        a0 = mfma(qf[s], wv[s], a0);
        a1 = mfma(qf[s], wv[4 + s], a1);
      }
      constexpr float SC = 0.08838834764831845f * 1.4426950408889634f;
      const float v0 = ((a0[0] + a0[1]) + (a0[2] + a0[3])) * SC, v1 = ((a1[0] + a1[1]) + (a1[2] + a1[3])) * SC;
      stc2(v2u{pk(v0, v1), tag}, rws, lane < 32 ? g * 65536 + n * 8 : OOB, WS_QA + h * 2048 + i * 512 + (w - 4) * 128);
    }
    bar0();
    __builtin_amdgcn_s_setprio(0);
    ST(5);
    if (G1_DELAY) __builtin_amdgcn_s_sleep(G1_DELAY);
    if (w < 3) touch(mk(wd, 1728 * 1280), L + L_DUMP, w * 64 + lane < 160 ? lane * 128 : OOB, (xcd * 16 + r) * 20480 + w * 8192);
    const int dh = i >> 1, ch = i & 1;
    {
      const rsrc_t rv = mk(wuv, 512 * 8192), ro = mk(wo, 4096 * 1280);
      const int lr = lane >> 3;
#pragma unroll
      for (int k = 0; k < 4; ++k)
        dma(rv, L + L1_UV + (4 * w + k) * 1024, lr * 8192 + (((lane & 7) ^ (((2 * w + (k >> 1)) & 3) << 1)) << 4),
            (256 * ch + 32 * w + 8 * k) * 8192 + h * 256 + dh * 128);
      const int ck = (lane & 7) ^ (((lr >> 1) & 3) << 1);
#pragma unroll
      for (int k = 0; k < 5; ++k)
        dma(ro, L + L1_WO + (5 * ch + k) * 8192 + w * 1024, lr * 1280 + ck * 16, (h * 128 + 64 * dh + 8 * w) * 1280 + (5 * ch + k) * 128);
      bar0();
#pragma unroll
      for (int k = 0; k < 5; ++k)
        dma(ro, L + L1_WO + (5 - 5 * ch + k) * 8192 + w * 1024, lr * 1280 + ck * 16, (h * 128 + 64 * dh + 8 * w) * 1280 + (5 - 5 * ch + k) * 128);
      SCHED();
    }
    ST(6);
    const int b = w >> 2, cb = w & 3;
    v2u fv[16], fo[20];
    {
      DRAIN();
      ST(7);
      bar();
      v4u ml;
      if (w == 0) ml = ldc(rws, lane * 512, WS_ML + h * 16);
      SCHED();
      {
        const int q = n >> 2, p = n & 3;
        const int rbv = (16 * q + g) * 128 + p * 8;
#pragma unroll
        for (int t = 0; t < 4; ++t)
#pragma unroll
          for (int e = 0; e < 4; ++e) fv[t * 4 + e] = trd(L + L1_UV + cb * 8192 + ((e & 1) * 8 + (e >> 1) * 4) * 128 + rbv + ((t ^ q) << 5));
        const int sw = 2 * (g & 1) + (q >> 1), rb = (4 * g + q) * 128 + p * 8;
#pragma unroll
        for (int t = 0; t < 5; ++t) {
          const int T = 5 * w + t;
#pragma unroll
          for (int e = 0; e < 4; ++e) fo[t * 4 + e] = trd(L + L1_WO + (T >> 2) * 8192 + e * 2048 + rb + (((T & 3) ^ sw) << 5));
        }
      }
      SCHED();
      if (w == 0) {
_Pragma("unroll 1")
        for (u32 it = 0; it < SPIN_CAP; ++it) {
          const u64 m1 = tagbad(ml, tag);
          if (!m1) break;
          reload1(ml, m1, lane * 512, dws, WS_ML + h * 16);
        }
        ST(11);
      }
      bar0();
      const int kg0 = 8 * g + 4 * (n >> 3);
      const int vo = kg0 * 1024 + (n & 7) * 16, so = WS_OB + (b * 32 + h) * 32768 + ch * 512 + cb * 128;
      v4u o0 = ldc(rws, vo, so), o1 = ldc(rws, vo + 1024, so), o2 = ldc(rws, vo + 2048, so), o3 = ldc(rws, vo + 3072, so);
      SCHED();
      if (w == 0) {
        const float m = __uint_as_float(ml.x);
        float M = fmaxf(m, dpp<XOR1>(m));
        M = fmaxf(M, dpp<XOR2>(M));
        M = fmaxf(M, dpp<ROR4>(M));
        M = fmaxf(M, dpp<ROR8>(M));
        M = x16_max(M);
        const float e = __builtin_amdgcn_exp2f(m - M);
        const u32 wb = pk(e, e);
        *(LAS u16*)(L + L1_W + lane * 2) = (u16)wb;
        float Ls = blo(wb) * __uint_as_float(ml.z);
        Ls += dpp<XOR1>(Ls);
        Ls += dpp<XOR2>(Ls);
        Ls += dpp<ROR4>(Ls);
        Ls += dpp<ROR8>(Ls);
        *(LAS float*)(L + L1_W + 128 + (lane >> 5) * 4) = __builtin_amdgcn_rcpf(x16_sum(Ls));
      }
      bar();
      const v2u w4 = *(LAS v2u*)(L + L1_W + b * 64 + kg0 * 2);
      const float inv = *(LAS float*)(L + L1_W + 128 + b * 4);
      v4u aw0 = selp(plo(w4.x, n), n), aw1 = selp(phi(w4.x, n), n), aw2 = selp(plo(w4.y, n), n), aw3 = selp(phi(w4.y, n), n);
      asm volatile("" : "+v"(aw0), "+v"(aw1), "+v"(aw2), "+v"(aw3));
      SCHED();
_Pragma("unroll 1")
      for (u32 it = 0; it < SPIN_CAP; ++it) {
        const u64 m0 = sentbad(o0), m1 = sentbad(o1), m2 = sentbad(o2), m3 = sentbad(o3);
        if (!(m0 | m1 | m2 | m3)) break;
        reload4<0, 1024, 2048, 3072>(o0, o1, o2, o3, m0, m1, m2, m3, vo, vo, vo, vo, dws, so);
      }
      ST(8);
      f4 acc = {0.f, 0.f, 0.f, 0.f};
      acc = mfma(aw0, o0, acc);
      acc = mfma(aw1, o1, acc);
      acc = mfma(aw2, o2, acc);
      acc = mfma(aw3, o3, acc);
      *(LAS v2u*)(L + L1_LA + w * 512 + (g >> 1) * 256 + n * 16 + (g & 1) * 8) = v2u{pk(acc[0] * inv, acc[1] * inv), pk(acc[2] * inv, acc[3] * inv)};
    }
    {
      const v4u a0 = *(LAS v4u*)(L + L1_LA + w * 512 + (n & 1) * 384 + g * 16), a1 = *(LAS v4u*)(L + L1_LA + w * 512 + (n & 1) * 384 + 64 + g * 16);
      float cv[4];
#pragma unroll
      for (int t = 0; t < 4; ++t) {
        f4 d = {0.f, 0.f, 0.f, 0.f};
        d = mfma(a0, v4u{fv[t * 4].x, fv[t * 4].y, fv[t * 4 + 1].x, fv[t * 4 + 1].y}, d);
        d = mfma(a1, v4u{fv[t * 4 + 2].x, fv[t * 4 + 2].y, fv[t * 4 + 3].x, fv[t * 4 + 3].y}, d);
        cv[t] = d[0] + d[1];
      }
      const float v01 = (g & 1) ? cv[1] : cv[0], v23 = (g & 1) ? cv[3] : cv[2], v = (g & 2) ? v23 : v01;
      *(LAS u16*)(L + L1_CX + w * 144 + (32 * (g >> 1) + 8 * (n >> 2) + 4 * (g & 1) + (n & 3)) * 2) = (u16)pk(v, v);
    }
    bar();
    ST(9);
    {
      const v4u a0 = *(LAS v4u*)(L + L1_CX + (n & 7) * 144 + g * 16), a1 = *(LAS v4u*)(L + L1_CX + (n & 7) * 144 + 64 + g * 16);
      u32 cv[5];
#pragma unroll
      for (int t = 0; t < 5; ++t) {
        f4 d = {0.f, 0.f, 0.f, 0.f};
        d = mfma(a0, v4u{fo[t * 4].x, fo[t * 4].y, fo[t * 4 + 1].x, fo[t * 4 + 1].y}, d);
        d = mfma(a1, v4u{fo[t * 4 + 2].x, fo[t * 4 + 2].y, fo[t * 4 + 3].x, fo[t * 4 + 3].y}, d);
        const u32 sv = __float_as_uint((d[0] + d[1]) + (d[2] + d[3]));
        auto sp = __builtin_amdgcn_permlane16_swap(sv, sv, false, false);
        cv[t] = pk(__uint_as_float(sp[0]), __uint_as_float(sp[1]));
      }
      const int so = WS_FA + 5 * w * 16384 + (h * 4 + i) * 128;
      const u32 c01 = (g & 1) ? cv[1] : cv[0], c23 = (g & 1) ? cv[3] : cv[2];
      stc2(v2u{(g & 2) ? c23 : c01, tag}, rws, g * 16384 + n * 8, so);
      stc2(v2u{cv[4], tag}, rws, lane < 16 ? n * 8 : OOB, so + 4 * 16384);
    }
    ST(10);
    STEND();
    __builtin_amdgcn_endpgm();
  }
  const int u = xcd * 16 + (r - 16), hg = r & 1, bk = u >> 1, b = bk >> 5, kg = bk & 31;
  {
    const int j = r - 16;
    if (w == 0) touch(mk(wq, 640 * 8192), L + L_DUMP, lane < 40 ? lane * 8192 : OOB, 40 * j * 8192 + xcd * 1024 + 384);
    if (HELP_UK == 1) touch(mk(wuk, 512 * 8192), L + L_DUMP, g * 8192 + (n >> 1) * 128, (32 * j + 4 * w) * 8192 + xcd * 1024);
    if (HELP_UK == 2) touch(mk(wuk, 512 * 8192), L + L_DUMP, g * 8192 + (n >> 2) * 256 + 128, (32 * j + 4 * w) * 8192 + xcd * 1024);
    if (HELP_UK == 0 && w == 2) touch(mk(wuk, 512 * 8192), L + L_DUMP, lane < 32 ? lane * 8192 : OOB, 32 * j * 8192 + xcd * 1024 + 384);
    if (w == 1) touch(mk(wuv, 512 * 8192), L + L_DUMP, lane < 32 ? lane * 8192 : OOB, 32 * j * 8192 + xcd * 1024 + 384);
    bar0();
  }
  u32 tag = __builtin_amdgcn_raw_buffer_load_b32(rws, 0, WS_EPOCH, 0);
  const int o_vo = WS_OB + (b * 32 + 16 * hg + 4 * g + 2 * (n >> 3)) * 32768 + kg * 1024 + w * 128 + (n & 7) * 16;
  const int fs = u >> 2, mq = u & 3;
  const int frow = 160 * mq + 20 * w + 5 * g;
  SCHED();
  if (G2_DELAY) __builtin_amdgcn_s_sleep(G2_DELAY);
  ST(1);
  {
    const rsrc_t rc = mk(ckv, 2 * 4096 * 1024);
#pragma unroll
    for (int k = 0; k < 8; ++k) dma(rc, L + (16 * w + k) * RS, lane * 16 + (k & 3) * 1024, (bk * 128 + 64 * hg + 8 * w + (k & 4)) * 1024);
    bar0();
#pragma unroll
    for (int k = 0; k < 8; ++k) dma(rc, L + (16 * w + 8 + k) * RS, lane * 16 + (k & 3) * 1024, (bk * 128 + 64 - 64 * hg + 8 * w + (k & 4)) * 1024);
    SCHED();
  }
  ST(2);
  float gate = 0.f;
  v3u xd;
  {
    const rsrc_t rx = mk(x, 2560), rg = mk(gw, 1280);
    xd = __builtin_amdgcn_raw_buffer_load_b96(rx, (((n >> 3) * 640 + frow) * 2) & ~3, 0, 0);
    if (w == 0 && u >= 88) {
      float s = 0.f;
#pragma unroll
      for (int k = 0; k < 5; ++k) {
        const v2u a = __builtin_amdgcn_raw_buffer_load_b64(rx, (lane >> 5) * 1280 + (lane & 31) * 8 + k * 256, 0, 0);
        const v2u c = __builtin_amdgcn_raw_buffer_load_b64(rg, (lane & 31) * 8 + k * 256, 0, 0);
        s += (blo(a.x) * blo(c.x) + bhi(a.x) * bhi(c.x)) + (blo(a.y) * blo(c.y) + bhi(a.y) * bhi(c.y));
      }
      s += dpp<XOR1>(s);
      s += dpp<XOR2>(s);
      s += dpp<ROR4>(s);
      s += dpp<ROR8>(s);
      gate = 1.f / (1.f + __expf(-x16_sum(s)));
    }
  }
  const int kt = w;
  v4u kf[16];
  {
    const int vo = lane * 16, so = WS_QA + (b * 32 + 16 * hg + 2 * w) * 2048;
    bar0();
    v4u q0 = ldc(rws, vo, so), q1 = ldc(rws, vo + 1024, so), q2 = ldc(rws, vo + 2048, so), q3 = ldc(rws, vo + 3072, so);
    SCHED();
    DRAIN();
    ST(3);
    tag += 1u;
#pragma unroll
    for (int s = 0; s < 16; ++s) kf[s] = *(LAS v4u*)(L + (16 * kt + n) * RS + s * 64 + g * 16);
    SCHED();
_Pragma("unroll 1")
    for (u32 it = 0; it < SPIN_CAP; ++it) {
      const u64 m0 = tagbad(q0, tag), m1 = tagbad(q1, tag), m2 = tagbad(q2, tag), m3 = tagbad(q3, tag);
      if (!(m0 | m1 | m2 | m3)) break;
      reload4<0, 1024, 2048, 3072>(q0, q1, q2, q3, m0, m1, m2, m3, vo, vo, vo, vo, dws, so);
    }
    LAS unsigned char* Q = L + L2_QF + 2 * w * RS + lane * 8;
    *(LAS v2u*)(Q) = v2u{q0.x, q0.z};
    *(LAS v2u*)(Q + 512) = v2u{q1.x, q1.z};
    *(LAS v2u*)(Q + RS) = v2u{q2.x, q2.z};
    *(LAS v2u*)(Q + RS + 512) = v2u{q3.x, q3.z};
  }
  ST(5);
  bar();
  v4u wv[5], dv[4];
  f4 sa = {0.f, 0.f, 0.f, 0.f};
  {
    const GAS unsigned char* wp = (const GAS unsigned char*)(n < 8 ? wg : wu) + frow * 3456 + (fs < 27 ? fs : 0) * 128 + (n & 7) * 16;
    LAS unsigned char* Q = L + L2_QF + n * RS + g * 16;
    v4u qf[16];
#pragma unroll
    for (int s = 0; s < 8; ++s) qf[s] = *(LAS v4u*)(Q + s * 64);
    SCHED();
#pragma unroll
    for (int s = 0; s < 16; ++s) {
      sa = mfma(kf[s], qf[s], sa);
      if (s < 8) qf[s + 8] = *(LAS v4u*)(Q + (s + 8) * 64);
      if (s % 3 == 0 && s < 15) wv[s / 3] = __builtin_nontemporal_load((const GAS v4u*)(wp + (s / 3) * 3456));
      SCHED();
    }
  }
  *(LAS float*)(L + L2_MX + n * 32 + w * 4) = x32_max(x16_max(fmaxf(fmaxf(sa[0], sa[1]), fmaxf(sa[2], sa[3]))));
  ST(6);
  bar();
  v2u vf[32];
  const int vrb = (4 * g + (n >> 2)) * RS + w * 128 + (n & 3) * 8;
  float M;
  {
    const f4 m0 = *(LAS f4*)(L + L2_MX + n * 32), m1 = *(LAS f4*)(L + L2_MX + n * 32 + 16);
    SCHED();
#pragma unroll
    for (int e = 0; e < 4; ++e)
#pragma unroll
      for (int t = 0; t < 4; ++t) vf[t * 8 + e] = trd(L + vrb + e * 16 * RS + t * 32);
    SCHED();
    M = fmaxf(fmaxf(fmaxf(m0[0], m0[1]), fmaxf(m0[2], m0[3])), fmaxf(fmaxf(m1[0], m1[1]), fmaxf(m1[2], m1[3])));
    const u32 p01 = pk(__builtin_amdgcn_exp2f(sa[0] - M), __builtin_amdgcn_exp2f(sa[1] - M));
    const u32 p23 = pk(__builtin_amdgcn_exp2f(sa[2] - M), __builtin_amdgcn_exp2f(sa[3] - M));
    *(LAS v2u*)(L + L2_PF + (kt >> 1) * 1024 + lane * 16 + (kt & 1) * 8) = v2u{p01, p23};
    *(LAS float*)(L + L2_LS + n * 32 + w * 4) = x32_sum(x16_sum((blo(p01) + bhi(p01)) + (blo(p23) + bhi(p23))));
  }
  bar();
  ST(7);
  {
    v4u pf[4];
#pragma unroll
    for (int s = 0; s < 4; ++s) pf[s] = *(LAS v4u*)(L + L2_PF + s * 1024 + lane * 16);
    SCHED();
#pragma unroll
    for (int e = 4; e < 8; ++e)
#pragma unroll
      for (int t = 0; t < 4; ++t) vf[t * 8 + e] = trd(L + vrb + e * 16 * RS + t * 32);
    SCHED();
    if (w == 0) {
      const f4 l0 = *(LAS f4*)(L + L2_LS + n * 32), l1 = *(LAS f4*)(L + L2_LS + n * 32 + 16);
      const float l = ((l0[0] + l0[1]) + (l0[2] + l0[3])) + ((l1[0] + l1[1]) + (l1[2] + l1[3]));
      stc(v4u{__float_as_uint(M), tag, __float_as_uint(l), tag}, rws, lane < 16 ? WS_ML + bk * 512 + (16 * hg + n) * 16 : OOB);
    }
    f4 o[4];
#pragma unroll
    for (int t = 0; t < 4; ++t) o[t] = f4{0.f, 0.f, 0.f, 0.f};
#pragma unroll
    for (int s = 0; s < 4; ++s)
#pragma unroll
      for (int t = 0; t < 4; ++t)
        o[t] = mfma(pf[s], v4u{vf[t * 8 + 2 * s].x, vf[t * 8 + 2 * s].y, vf[t * 8 + 2 * s + 1].x, vf[t * 8 + 2 * s + 1].y}, o[t]);
    u32 pa[4], pb[4];
#pragma unroll
    for (int k = 0; k < 4; ++k) {
      pa[k] = min(pk(o[0][k], o[1][k]), SENT - 1u);
      pb[k] = min(pk(o[2][k], o[3][k]), SENT - 1u);
    }
#pragma unroll
    for (int k = 0; k < 2; ++k) {
      v4u pc;
      pc.x = __builtin_amdgcn_update_dpp(pa[k], pa[2 + k], ROR8, 0xf, 0xc, false);
      pc.y = __builtin_amdgcn_update_dpp(pb[k], pb[2 + k], ROR8, 0xf, 0xc, false);
      pc.z = __builtin_amdgcn_update_dpp(pa[2 + k], pa[k], ROR8, 0xf, 0x3, false);
      pc.w = __builtin_amdgcn_update_dpp(pb[2 + k], pb[k], ROR8, 0xf, 0x3, false);
      stc(pc, rws, o_vo + k * 32768);
    }
  }
  ST(8);
  if (u < 108) {
    bar0();
    {
      const rsrc_t rd = mk(wd, 1728 * 1280);
      const int so = w < 5 ? (64 * fs + 16 * mq) * 1280 + w * 256 : OOB;
#pragma unroll
      for (int t = 0; t < 4; ++t) dv[t] = ldw(rd, g * 4 * 1280 + n * 16 + t * 1280, so);
      SCHED();
    }
    {
      const u32 sh = (g & 1) << 4;
      const u32 a0 = __builtin_amdgcn_alignbit(xd.y, xd.x, sh), a1 = __builtin_amdgcn_alignbit(xd.z, xd.y, sh), a2 = xd.z >> sh;
      f4 acc = {0.f, 0.f, 0.f, 0.f};
      acc = mfma(selp(plo(a0, n), n), wv[0], acc);
      acc = mfma(selp(phi(a0, n), n), wv[1], acc);
      acc = mfma(selp(plo(a1, n), n), wv[2], acc);
      acc = mfma(selp(phi(a1, n), n), wv[3], acc);
      acc = mfma(selp(plo(a2, n), n), wv[4], acc);
      *(LAS f4*)(L + L2_QF + w * 1024 + lane * 16) = acc;
    }
    bar();
    if (w == 7) {
      f4 p[8];
#pragma unroll
      for (int k = 0; k < 8; ++k) p[k] = *(LAS f4*)(L + L2_QF + k * 1024 + lane * 16);
      SCHED();
      f4 s = ((p[0] + p[1]) + (p[2] + p[3])) + ((p[4] + p[5]) + (p[6] + p[7]));
      ST(9);
      s = exchange4(s, rws, dws, 512 + u, lane, tag);
      float hv[4];
#pragma unroll
      for (int k = 0; k < 4; ++k) hv[k] = s[k] * dpp<ROR8>(s[k]) * __builtin_amdgcn_rcpf(1.f + __expf(-s[k]));
      *(LAS v2u*)(L + L2_H + (g >> 1) * 256 + n * 16 + (g & 1) * 8) = v2u{pk(hv[0], hv[1]), pk(hv[2], hv[3])};
      ST(10);
    }
    bar();
    if (w < 5) {
      const v2u hh = *(LAS v2u*)(L + L2_H + (n >> 3) * 256 + mq * 32 + g * 8);
      f4 d = {0.f, 0.f, 0.f, 0.f};
      d = mfma(selp(plo(hh.x, n), n), dv[0], d);
      d = mfma(selp(phi(hh.x, n), n), dv[1], d);
      d = mfma(selp(plo(hh.y, n), n), dv[2], d);
      d = mfma(selp(phi(hh.y, n), n), dv[3], d);
      stc(v4u{pk(d[0], d[1]), tag, pk(d[2], d[3]), tag}, rws, WS_FF + u * 5120 + w * 512 + (g >> 1) * 2560 + n * 32 + (g & 1) * 16);
    }
    ST(11);
  }
  if (u >= 88) {
    const int T = u - 88;
    int v0, v3, st, so;
    if (w < 4) {
      so = WS_FA + T * 16384 + w * 4096;
      v0 = lane * 16;
      st = 1024;
      v3 = v0 + 3072;
    } else {
      so = WS_FF + T * 64 + (w - 4) * 27 * 5120;
      v0 = (lane >> 3) * 5120 + ((lane >> 2) & 1) * 2560 + (lane & 3) * 16;
      st = 8 * 5120;
      v3 = lane < 24 ? v0 + 24 * 5120 : v0;
    }
    const int v1 = v0 + st, v2 = v1 + st;
    if (FA_REP) {
      v4u a = ldc(rws, 0, so);
_Pragma("unroll 1")
      for (u32 it = 0; it < SPIN_CAP; ++it) {
        if (!tagbad(a, tag)) break;
        reload1(a, ~0ull, 0, dws, so);
      }
    }
    ST(12);
    POLL();
    v4u a0 = ldc(rws, v0, so), a1 = ldc(rws, v1, so), a2 = ldc(rws, v2, so), a3 = ldc(rws, v3, so);
_Pragma("unroll 1")
    for (u32 it = 0; it < SPIN_CAP; ++it) {
      const u64 m0 = tagbad(a0, tag), m1 = tagbad(a1, tag), m2 = tagbad(a2, tag), m3 = tagbad(a3, tag);
      if (!(m0 | m1 | m2 | m3)) break;
      reload4<0, 0, 0, 0>(a0, a1, a2, a3, m0, m1, m2, m3, v0, v1, v2, v3, dws, so);
    }
    ST(13);
    const u32 keep = (w >= 4 && lane >= 24) ? 0u : ~0u;
    a3.x &= keep;
    a3.z &= keep;
    f4 v = (unpk(a0) + unpk(a1)) + (unpk(a2) + unpk(a3));
#pragma unroll
    for (int k = 0; k < 4; ++k) {
      float s = v[k];
      s += dpp<ROR8>(s);
      v[k] = x32_sum(x16_sum(s));
    }
    *(LAS f4*)(L + L2_FR + w * 128 + (lane & 7) * 16) = v;
    bar();
    if (w == 0) {
      const int bb = lane >> 5;
      float A = 0.f, F = 0.f;
#pragma unroll
      for (int k = 0; k < 4; ++k) {
        A += *(LAS float*)(L + L2_FR + k * 128 + n * 8 + bb * 4);
        F += *(LAS float*)(L + L2_FR + 512 + k * 128 + bb * 64 + n * 4);
      }
      const float o = gate * A + (1.f - gate) * F;
      __builtin_amdgcn_raw_buffer_store_b16((u16)pk(o, o), mk(out, 2560), (lane & 16) ? OOB : bb * 1280 + n * 2, T * 32, 0);
      if (T == 0) stc1(tag, rws, lane == 0 ? WS_EPOCH : OOB, 0);
    }
    ST(14);
  }
  STEND();
}

extern "C" void kernel_launch(void* const* d_in, const int* in_sizes, int n_in, void* d_out, int out_size, void* d_ws, size_t ws_size,
                              hipStream_t stream) {
  mla_ffn_mix<<<256, 512, 0, stream>>>((const u16*)d_in[0], (const u16*)d_in[1], (const u16*)d_in[2], (const u16*)d_in[3],
                                       (const u16*)d_in[4], (const u16*)d_in[5], (const u16*)d_in[7], (const u16*)d_in[8],
                                       (const u16*)d_in[9], (const u16*)d_in[10], (u16*)d_out, (unsigned char*)d_ws);
}
```

```cpp
#include <hip/hip_runtime.h>
#include <stdint.h>

typedef unsigned u32;
typedef unsigned long long u64;
typedef unsigned short u16;
typedef u32 v4u __attribute__((ext_vector_type(4)));
typedef u32 v3u __attribute__((ext_vector_type(3)));
typedef u32 v2u __attribute__((ext_vector_type(2)));
typedef float f4 __attribute__((ext_vector_type(4)));
typedef __bf16 bf8 __attribute__((ext_vector_type(8)));
typedef __bf16 bf2 __attribute__((ext_vector_type(2)));
typedef float f2 __attribute__((ext_vector_type(2)));
typedef short s4 __attribute__((ext_vector_type(4)));
typedef short s2 __attribute__((ext_vector_type(2)));
#define GAS __attribute__((address_space(1)))
#define LAS __attribute__((address_space(3)))
typedef __amdgpu_buffer_rsrc_t rsrc_t;

constexpr int AUX_SC1 = 16;
#define CODE_BYTES 13056
#define G2_DELAY 24
#define G1_DELAY 28
#define QG_DELAY 0
#define FA_REP 1
#define BADCOL 1
#define PROBE_THR 130
#define UK_LAST 1
#define HMAP 1
#define CLEAN_DELAY 0
#define CKV_ORDER 0
#define QA_TOUCH 1
#define FFN_TOUCH 1
#define HEADSUP 0
#define PACE 63
constexpr u32 SPIN_CAP = 1u << 16;
constexpr u32 SENT = 0xFFFFFFFFu;
constexpr int OOB = 0x40000000;

constexpr int WS_EPOCH = 0;
constexpr int WS_ML = 1024;
constexpr int WS_HU = 40960;
constexpr int WS_QP = 65536;
constexpr int WS_QA = WS_QP + 128 * 1024;
constexpr int WS_FF = WS_QA + 256 * 1024;
constexpr int WS_FA = WS_FF + 108 * 5120;
constexpr int WS_OB = 2 * 1024 * 1024;
constexpr int WS_END = WS_OB + 2 * 1024 * 1024;
static_assert(WS_FA + 40 * 128 * 128 <= WS_OB, "");

constexpr int RS = 1056;
constexpr int L2_QF = 128 * RS;
constexpr int L2_PF = L2_QF + 16 * RS;
constexpr int L2_MX = L2_PF + 4096;
constexpr int L2_FR = L2_MX + 1280;
constexpr int L2_H = L2_FR + 1024;
constexpr int L_DUMP = L2_H + 512;
constexpr int LDS_BYTES = L_DUMP + 512;
constexpr int L1_UV = 0, L1_WO = 32768, L1_X = L1_WO + 81920, L1_LA = L1_X + 8192, L1_CX = L1_LA + 4 * 528, L1_Q = L1_CX + 4 * 144,
              L1_W = L1_Q + 16 * 144, L1_H = L1_W + 128;
static_assert(L1_H + 512 <= L_DUMP && LDS_BYTES <= 163840, "");

__device__ __forceinline__ f4 mfma(v4u a, v4u b, f4 c) {
  return __builtin_amdgcn_mfma_f32_16x16x32_bf16(__builtin_bit_cast(bf8, a), __builtin_bit_cast(bf8, b), c, 0, 0, 0);
}
__device__ __forceinline__ f4 mfma16(v2u a, v2u b, f4 c) {
  return __builtin_amdgcn_mfma_f32_16x16x16bf16_1k(__builtin_bit_cast(s4, a), __builtin_bit_cast(s4, b), c, 0, 0, 0);
}
__device__ __forceinline__ u32 pk(float lo, float hi) {
  f2 v = {lo, hi};
  return __builtin_bit_cast(u32, __builtin_convertvector(v, bf2));
}
__device__ __forceinline__ float blo(u32 v) { return __uint_as_float(v << 16); }
__device__ __forceinline__ float bhi(u32 v) { return __uint_as_float(v & 0xFFFF0000u); }
template <int CTRL> __device__ __forceinline__ float dpp(float x) {
  return __builtin_bit_cast(float, __builtin_amdgcn_mov_dpp(__builtin_bit_cast(int, x), CTRL, 0xf, 0xf, true));
}
constexpr int XOR1 = 0xB1, XOR2 = 0x4E, ROR8 = 0x128, ROR4 = 0x124;
__device__ __forceinline__ float max3(float a, float b, float c) {
  float r;
  asm("v_max3_f32 %0, %1, %2, %3" : "=v"(r) : "v"(a), "v"(b), "v"(c));
  return r;
}
__device__ __forceinline__ float max2(float a, float b) {
  float r;
  asm("v_max_f32 %0, %1, %2" : "=v"(r) : "v"(a), "v"(b));
  return r;
}
__device__ __forceinline__ float x16_max(float x) {
  auto s = __builtin_amdgcn_permlane16_swap(__float_as_uint(x), __float_as_uint(x), false, false);
  return fmaxf(__uint_as_float(s[0]), __uint_as_float(s[1]));
}
__device__ __forceinline__ float x16_sum(float x) {
  auto s = __builtin_amdgcn_permlane16_swap(__float_as_uint(x), __float_as_uint(x), false, false);
  return __uint_as_float(s[0]) + __uint_as_float(s[1]);
}
__device__ __forceinline__ float x32_max(float x) {
  auto t = __builtin_amdgcn_permlane32_swap(__float_as_uint(x), __float_as_uint(x), false, false);
  return fmaxf(__uint_as_float(t[0]), __uint_as_float(t[1]));
}
__device__ __forceinline__ rsrc_t mk(const void* p, u32 bytes) { return __builtin_amdgcn_make_buffer_rsrc((void*)p, 0, bytes, 0x00020000); }
__device__ __forceinline__ v4u mkd(const void* p, u32 bytes) {
  return v4u{(u32)(size_t)p, (u32)((size_t)p >> 32) & 0xFFFFu, bytes, 0x00020000u};
}
__device__ __forceinline__ v4u ldc(rsrc_t r, int vo, int so) { return __builtin_amdgcn_raw_buffer_load_b128(r, vo, so, AUX_SC1); }
__device__ __forceinline__ void stc(v4u v, rsrc_t r, int vo) { __builtin_amdgcn_raw_buffer_store_b128(v, r, vo, 0, AUX_SC1); }
__device__ __forceinline__ void stc2(v2u v, rsrc_t r, int vo, int so) { __builtin_amdgcn_raw_buffer_store_b64(v, r, vo, so, AUX_SC1); }
__device__ __forceinline__ void stc1(u32 v, rsrc_t r, int vo, int so) { __builtin_amdgcn_raw_buffer_store_b32(v, r, vo, so, AUX_SC1); }
template <int N, int SSTEP, int LSTEP, int KEEP = 63> __device__ __forceinline__ void dma_run(v4u d, int vo, int so, int lds) {
  int t;
  asm volatile("s_mov_b32 m0, %[l]\n\ts_mov_b32 %[t], %[s]\n\t.rept %[n]\n\tbuffer_load_dwordx4 %[v], %[d], %[t] offen lds\n\t"
               "s_add_u32 m0, m0, %[ls]\n\ts_add_u32 %[t], %[t], %[ss]\n\t.if %[k] < 63\n\ts_waitcnt vmcnt(%[k])\n\t.endif\n\t.endr"
               : [t] "=&s"(t)
               : [v] "v"(vo), [d] "s"(d), [s] "s"(so), [l] "s"(lds), [n] "i"(N), [ls] "i"(LSTEP), [ss] "i"(SSTEP), [k] "i"(KEEP)
               : "memory", "scc");
}
template <int N, int SSTEP, int LSTEP> __device__ __forceinline__ void dma_run2(v4u d, int va, int vb, int so, int lds) {
  int t;
  asm volatile("s_mov_b32 m0, %[l]\n\ts_mov_b32 %[t], %[s]\n\t.rept %[n]\n\tbuffer_load_dwordx4 %[va], %[d], %[t] offen lds\n\t"
               "s_add_u32 m0, m0, 0x400\n\ts_nop 0\n\tbuffer_load_dwordx4 %[vb], %[d], %[t] offen lds\n\t"
               "s_add_u32 m0, m0, %[ls]\n\ts_add_u32 %[t], %[t], %[ss]\n\t.endr"
               : [t] "=&s"(t)
               : [va] "v"(va), [vb] "v"(vb), [d] "s"(d), [s] "s"(so), [l] "s"(lds), [n] "i"(N), [ls] "i"(LSTEP - 1024), [ss] "i"(SSTEP)
               : "memory", "scc");
}
template <int N, int SSTEP, int LSTEP, int LD> __device__ __forceinline__ void dma_zip(v4u d, int vo, int sa, int sb, int lds) {
  int t, t2;
  asm volatile("s_mov_b32 m0, %[l]\n\ts_mov_b32 %[t], %[sa]\n\ts_mov_b32 %[u], %[sb]\n\t.rept %[n]\n\t"
               "buffer_load_dwordx4 %[v], %[d], %[t] offen lds\n\ts_add_u32 m0, m0, %[ld]\n\ts_add_u32 %[t], %[t], %[ss]\n\t"
               "buffer_load_dwordx4 %[v], %[d], %[u] offen lds\n\ts_sub_u32 m0, m0, %[lb]\n\ts_add_u32 %[u], %[u], %[ss]\n\t.endr"
               : [t] "=&s"(t), [u] "=&s"(t2)
               : [v] "v"(vo), [d] "s"(d), [sa] "s"(sa), [sb] "s"(sb), [l] "s"(lds), [n] "i"(N), [ld] "i"(LD), [lb] "i"(LD - LSTEP), [ss] "i"(SSTEP)
               : "memory", "scc");
}
__device__ __forceinline__ void touch(v4u d, int vo, int so, int lds) {
  asm volatile("s_mov_b32 m0, %[l]\n\ts_nop 0\n\tbuffer_load_dword %[v], %[d], %[s] offen lds" ::[v] "v"(vo), [d] "s"(d), [s] "s"(so), [l] "s"(lds)
               : "memory");
}
__device__ __forceinline__ v2u trd(LAS unsigned char* p) {
  return __builtin_bit_cast(v2u, __builtin_amdgcn_ds_read_tr16_b64_v4i16((LAS s4*)p));
}
__device__ __forceinline__ v4u cat(v2u a, v2u b) { return v4u{a.x, a.y, b.x, b.y}; }
#define SCHED() __builtin_amdgcn_sched_barrier(0)
#define PIN(x) asm volatile("" : "+v"(x))
#define PINS(x) asm volatile("" : "+s"(x))
__device__ __forceinline__ void bar() {
  asm volatile("s_waitcnt lgkmcnt(0)" ::: "memory");
  __builtin_amdgcn_s_barrier();
  asm volatile("" ::: "memory");
}
__device__ __forceinline__ void bar0() {
  SCHED();
  asm volatile("" ::: "memory");
  __builtin_amdgcn_s_barrier();
  asm volatile("" ::: "memory");
  SCHED();
}
#define VMW(n) asm volatile("s_waitcnt vmcnt(" #n ")" ::: "memory")
#define DRAIN() VMW(0)
__device__ __forceinline__ u64 tagbad(v4u a, u32 tag) { return __builtin_amdgcn_ballot_w64(a.y != tag) | __builtin_amdgcn_ballot_w64(a.w != tag); }
__device__ __forceinline__ u64 sentbad(v4u a) { return __builtin_amdgcn_ballot_w64(max(max(a.x, a.y), max(a.z, a.w)) == SENT); }
#define CX(i) "v_cmpx_eq_u32 vcc, %[t], %[a" #i "]\n\t"
__device__ __forceinline__ bool alltag(u32 tag, v4u p, v4u q, v4u r, v4u s) {
  u64 e;
  asm volatile(CX(0) CX(1) CX(2) CX(3) CX(4) CX(5) CX(6) CX(7) "s_mov_b64 %[e], exec\n\ts_mov_b64 exec, -1\n\ts_nop 1"
               : [e] "=s"(e)
               : [t] "s"(tag), [a0] "v"(p.y), [a1] "v"(p.w), [a2] "v"(q.y), [a3] "v"(q.w), [a4] "v"(r.y), [a5] "v"(r.w), [a6] "v"(s.y), [a7] "v"(s.w)
               : "vcc");
  return e == ~0ull;
}
#define RL1(i) "s_mov_b64 exec, %[m" #i "]\n\ts_cbranch_execz " #i "f\n\tbuffer_load_dwordx4 %[a" #i "], %[v" #i "], %[rs], %[so] offen offset:%[o" #i "] sc1\n" #i ":\n\t"
__device__ __forceinline__ void reload4(v4u& a1, v4u& a2, v4u& a3, v4u& a4, u64 m1, u64 m2, u64 m3, u64 m4, int v, v4u rs, int so) {
  asm volatile("s_nop 4\n\t" RL1(1) RL1(2) RL1(3) RL1(4) "s_mov_b64 exec, -1\n\ts_waitcnt vmcnt(0)"
               : [a1] "+v"(a1), [a2] "+v"(a2), [a3] "+v"(a3), [a4] "+v"(a4)
               : [m1] "s"(m1), [m2] "s"(m2), [m3] "s"(m3), [m4] "s"(m4), [v1] "v"(v), [v2] "v"(v), [v3] "v"(v), [v4] "v"(v), [rs] "s"(rs),
                 [so] "s"(so), [o1] "i"(0), [o2] "i"(1024), [o3] "i"(2048), [o4] "i"(3072)
               : "memory");
}
__device__ __forceinline__ void reload3(v4u& a1, v4u& a2, v4u& a3, u64 m1, u64 m2, u64 m3, int v1, int v2, int v3, v4u rs, int so) {
  asm volatile("s_nop 4\n\t" RL1(1) RL1(2) RL1(3) "s_mov_b64 exec, -1\n\ts_waitcnt vmcnt(0)"
               : [a1] "+v"(a1), [a2] "+v"(a2), [a3] "+v"(a3)
               : [m1] "s"(m1), [m2] "s"(m2), [m3] "s"(m3), [v1] "v"(v1), [v2] "v"(v2), [v3] "v"(v3), [rs] "s"(rs), [so] "s"(so), [o1] "i"(0),
                 [o2] "i"(0), [o3] "i"(0)
               : "memory");
}
__device__ __forceinline__ void reload1(v4u& a1, u64 m1, int v1, v4u rs, int so) {
  asm volatile("s_nop 4\n\t" RL1(1) "s_mov_b64 exec, -1\n\ts_waitcnt vmcnt(0)"
               : [a1] "+v"(a1)
               : [m1] "s"(m1), [v1] "v"(v1), [rs] "s"(rs), [so] "s"(so), [o1] "i"(0)
               : "memory");
}
__device__ __forceinline__ void reload4v(v4u& a1, v4u& a2, v4u& a3, v4u& a4, u64 m1, u64 m2, u64 m3, u64 m4, int v1, int v2, int v3, int v4, v4u rs,
                                         int so) {
  asm volatile("s_nop 4\n\t" RL1(1) RL1(2) RL1(3) RL1(4) "s_mov_b64 exec, -1\n\ts_waitcnt vmcnt(0)"
               : [a1] "+v"(a1), [a2] "+v"(a2), [a3] "+v"(a3), [a4] "+v"(a4)
               : [m1] "s"(m1), [m2] "s"(m2), [m3] "s"(m3), [m4] "s"(m4), [v1] "v"(v1), [v2] "v"(v2), [v3] "v"(v3), [v4] "v"(v4), [rs] "s"(rs),
                 [so] "s"(so), [o1] "i"(0), [o2] "i"(0), [o3] "i"(0), [o4] "i"(0)
               : "memory");
}
__device__ __forceinline__ void settle4(v4u& a, v4u& b, v4u& c, v4u& d, u32 tag, int v, v4u rs, int so) {
  if (__builtin_expect(!alltag(tag, a, b, c, d), 0)) {
#pragma unroll 1
    for (u32 it = 0; it < SPIN_CAP; ++it) {
      const u64 m0 = tagbad(a, tag), m1 = tagbad(b, tag), m2 = tagbad(c, tag), m3 = tagbad(d, tag);
      if (!(m0 | m1 | m2 | m3)) break;
      __builtin_amdgcn_s_sleep(2);
      reload4(a, b, c, d, m0, m1, m2, m3, v, rs, so);
    }
  }
}
__device__ __forceinline__ v4u sel(u32 dup, v4u m) { return v4u{dup & m.x, dup & m.y, dup & m.z, dup & m.w}; }
__device__ __forceinline__ v4u selmask(int n) {
  const u32 v = 0xFFFFu << ((n & 1) << 4);
  const int q = (n >> 1) & 3;
  return v4u{q == 0 ? v : 0u, q == 1 ? v : 0u, q == 2 ? v : 0u, q == 3 ? v : 0u};
}
__device__ __forceinline__ u32 duplo(u32 d) { return __builtin_amdgcn_perm(d, d, 0x01000100u); }
__device__ __forceinline__ u32 duphi(u32 d) { return __builtin_amdgcn_perm(d, d, 0x03020302u); }
__device__ __forceinline__ f4 unpk(v4u a) { return f4{blo(a.x), bhi(a.x), blo(a.z), bhi(a.z)}; }

__device__ __forceinline__ f4 exchange4(f4 s, rsrc_t rws, v4u dws, int slot, int lane, u32 tag) {
  stc(v4u{pk(s[0], s[1]), tag, pk(s[2], s[3]), tag}, rws, WS_QP + slot * 1024 + lane * 16);
  const int v0 = (slot & 3) * 1024 + lane * 16, so = WS_QP + (slot & ~3) * 1024;
  DRAIN();
  v4u q1 = ldc(rws, v0 ^ 1024, so), q2 = ldc(rws, v0 ^ 2048, so), q3 = ldc(rws, v0 ^ 3072, so);
#pragma unroll 1
  for (u32 it = 0; it < SPIN_CAP; ++it) {
    const u64 m1 = tagbad(q1, tag), m2 = tagbad(q2, tag), m3 = tagbad(q3, tag);
    if (!(m1 | m2 | m3)) break;
    __builtin_amdgcn_s_sleep(2);
    reload3(q1, q2, q3, m1, m2, m3, v0 ^ 1024, v0 ^ 2048, v0 ^ 3072, dws, so);
  }
  return (s + unpk(q1)) + (unpk(q2) + unpk(q3));
}

__device__ __forceinline__ float mix_gate(const u16* x, const u16* gw, int lane) {
  const rsrc_t rx = mk(x, 2560), rg = mk(gw, 1280);
  v2u a[5], c[5];
#pragma unroll
  for (int k = 0; k < 5; ++k) {
    a[k] = __builtin_amdgcn_raw_buffer_load_b64(rx, (lane >> 5) * 1280 + (lane & 31) * 8 + k * 256, 0, 0);
    c[k] = __builtin_amdgcn_raw_buffer_load_b64(rg, (lane & 31) * 8 + k * 256, 0, 0);
  }
  float s = 0.f;
#pragma unroll
  for (int k = 0; k < 5; ++k) s += (blo(a[k].x) * blo(c[k].x) + bhi(a[k].x) * bhi(c[k].x)) + (blo(a[k].y) * blo(c[k].y) + bhi(a[k].y) * bhi(c[k].y));
  s += dpp<XOR1>(s);
  s += dpp<XOR2>(s);
  s += dpp<ROR4>(s);
  s += dpp<ROR8>(s);
  return 1.f / (1.f + __expf(-x16_sum(s)));
}

#define ST(i)
#define STEND()

extern "C" __global__ void __launch_bounds__(512, 2)
mla_ffn_mix(const u16* __restrict__ x, const u16* __restrict__ ckv, const u16* __restrict__ wq, const u16* __restrict__ wuk,
            const u16* __restrict__ wuv, const u16* __restrict__ wo, const u16* __restrict__ wg, const u16* __restrict__ wu,
            const u16* __restrict__ wd, const u16* __restrict__ gw, u16* __restrict__ out, unsigned char* __restrict__ ws) {
  __shared__ __attribute__((aligned(16))) unsigned char smem[LDS_BYTES];
  LAS unsigned char* L = (LAS unsigned char*)smem;
  const int LB = (int)(u32)(size_t)L;
  ST(0);
  const int tid = threadIdx.x, bid = blockIdx.x;
  u64 kbase;
  asm volatile("s_getpc_b64 %0\n\ts_andn2_b64 %0, %0, 0xff" : "=s"(kbase)::"scc");
  const int lane = tid & 63, w = __builtin_amdgcn_readfirstlane(tid >> 6);
  const int xcd = bid & 7, r = bid >> 3;
  const int n = lane & 15, g = lane >> 4;
  const int h = (HMAP ? (xcd & 3) * 8 + (xcd & 4) : 4 * xcd) + (r >> 2), i = r & 3, dh = i >> 1, ch = i & 1;
  const int lr = lane >> 3, ckx = ((lane & 7) ^ (lr & 6)) << 4;
  const int col = h * 256 + dh * 128, r0 = 80 * (w ^ (4 * ch));
  int qva = ((lr >> 2) * 20 + (lr & 3)) * 8192 + ckx, qvb = qva + 40 * 8192, qso = r0 * 8192 + col, qld = LB + L1_WO + w * 10240;
  PIN(qva); PIN(qvb); PINS(qso); PINS(qld);
  SCHED();
  asm volatile("" ::"s"(x), "s"(ckv), "s"(wq), "s"(wuk), "s"(wuv), "s"(wo), "s"(wg), "s"(wu), "s"(wd), "s"(gw), "s"(out), "s"(ws));
  const v4u dq = mkd(wq, 640 * 8192);
  if (bid < 128 && w < 4) dma_run2<5, 4 * 8192, 2048>(dq, qva, qvb, qso, qld);
  SCHED();
  u32 epoch, tag;
  asm volatile("s_load_dword %0, %1, 0x0" : "=s"(epoch) : "s"(ws) : "memory");
#define GET_TAG()                                                                          \
  do {                                                                                     \
    asm volatile("s_waitcnt lgkmcnt(0)" : "+s"(epoch)::"memory");                          \
    epoch += 1u;                                                                           \
    tag = (epoch & 0x3FFFu) | ((epoch << 1) & 0x3FFF8000u) | ((epoch << 2) & 0x80000000u); \
    PINS(tag);                                                                             \
  } while (0)
  const rsrc_t rws = mk(ws, WS_END);
  const v4u dws = mkd(ws, WS_END);

  if (__builtin_expect(r < 16, 1)) {
    const int q = n >> 2, p = n & 3, sw = 2 * (g & 1) + (q >> 1), rb = (4 * g + q) * 128 + p * 8;
    {
      const v4u dk = mkd(wuk, 512 * 8192);
      int ta[4];
#pragma unroll
      for (int t = 0; t < 4; ++t) {
        ta[t] = L1_WO + w * 10240 + rb + ((t ^ sw) << 5);
        PIN(ta[t]);
      }
      int kvo = lr * 8192 + ckx, xvo = (lane >> 5) * 1280 + (lane & 31) * 16;
      PIN(kvo); PIN(xvo);
      bar0();
      if (w >= 4) dma_run2<5, 4 * 8192, 2048>(dq, qva, qvb, qso, qld);
      dma_run<1, 0, 0>(mkd(x, 2560), xvo, r0 * 2, LB + L1_X + w * 1024);
      GET_TAG();
      int qw = L1_Q + (8 * (g >> 1) + w) * 144 + (g & 1) * 64 + n * 2, qr = L1_Q + n * 144 + g * 16;
      int svo = ((lane & 16) << 26) | ((g >> 1) * 65536 + n * 8), sso = WS_QA + dh * 131072 + h * 2048 + ch * 1024 + w * 128;
      int xr = L1_X + w * 1024 + (n >> 3) * 512 + g * 40, ur0 = L1_UV + w * 4096 + n * 256 + ((g ^ (p << 1)) << 4), ur1 = ur0 ^ 64;
      PIN(qw); PIN(qr); PIN(svo); PINS(sso); PIN(xr); PIN(ur0); PIN(ur1);
      if (UK_LAST) bar0();
      dma_run<4, 8 * 8192, 1024>(dk, kvo, (256 * ch + 32 * w) * 8192 + col, LB + L1_UV + w * 4096);
      ST(1);
      VMW(4);
      v2u xf[5], f[20];
#pragma unroll
      for (int a = 0; a < 5; ++a) {
        xf[a] = *(LAS v2u*)(L + xr + a * 8);
#pragma unroll
        for (int t = 0; t < 4; ++t) f[a * 4 + t] = trd(L + ta[t] + a * 2048);
      }
      SCHED();
      f4 d[4];
#pragma unroll
      for (int t = 0; t < 4; ++t) d[t] = f4{0.f, 0.f, 0.f, 0.f};
#pragma unroll
      for (int a = 0; a < 5; ++a)
#pragma unroll
        for (int t = 0; t < 4; ++t) d[t] = mfma16(xf[a], f[a * 4 + t], d[t]);
      const u32 qq = pk((g & 1) ? d[2][0] : d[0][0], (g & 1) ? d[3][0] : d[1][0]);
      *(LAS u16*)(L + qw) = (u16)qq;
      *(LAS u16*)(L + qw + 32) = (u16)(qq >> 16);
      ST(2);
      SCHED();
      VMW(0);
      v4u uk[4];
#pragma unroll
      for (int k = 0; k < 4; ++k) uk[k] = *(LAS v4u*)(L + ((k & 1) ? ur1 : ur0) + (k >> 1) * 128);
      ST(12);
      bar();
      const v4u q0 = *(LAS v4u*)(L + qr), q1 = *(LAS v4u*)(L + qr + 64);
      f4 a0 = mfma(q0, uk[0], f4{0.f, 0.f, 0.f, 0.f});
      f4 a1 = mfma(q0, uk[2], f4{0.f, 0.f, 0.f, 0.f});
      a0 = mfma(q1, uk[1], a0);
      a1 = mfma(q1, uk[3], a1);
      constexpr float SC = 0.08838834764831845f * 1.4426950408889634f;
      const float v0 = x16_sum((a0[0] + a0[1]) + (a0[2] + a0[3])) * SC, v1 = x16_sum((a1[0] + a1[1]) + (a1[2] + a1[3])) * SC;
      stc2(v2u{pk(v0, v1), tag}, rws, svo, sso);
    }
    bar0();
    ST(5);
    float gate = 0.f;
    if (w == 7) {
      gate = mix_gate(x, gw, lane);
      PIN(gate);
    } else if (G1_DELAY) {
      __builtin_amdgcn_s_sleep(G1_DELAY);
    }
    bar0();
    const int b = w >> 2, cb = w & 3;
    if (QA_TOUCH && w == 0) touch(dws, lane * 128, WS_QA + (r >> 3) * 131072 + (xcd >> 2) * 65536 + (r & 7) * 8192, LB + L_DUMP);
    if (FFN_TOUCH && xcd * 16 + r < 108) {
      const int fu = xcd * 16 + r;
      if (w >= 1 && w < 7) {
        const int k = w - 1 - (w >= 4 ? 3 : 0), rr = k * 64 + lane;
        touch(mkd(w >= 4 ? wu : wg, 640 * 3456), rr < 160 ? rr * 3456 : OOB, 160 * i * 3456 + (fu >> 2) * 128, LB + L_DUMP);
      }
    }
    {
      const int uvo = lr * 8192 + (((lane & 7) ^ (w & 6)) << 4), ovo = lr * 1280 + ckx;
      dma_run<4, 64 * 8192, 8192, PACE>(mkd(wuv, 512 * 8192), uvo, (256 * ch + 8 * w) * 8192 + col, LB + L1_UV + w * 1024);
      const v4u dwo = mkd(wo, 4096 * 1280);
      const int oso = (h * 128 + 64 * dh + 8 * w) * 1280;
      dma_run<5, 128, 8192, PACE>(dwo, ovo, oso + ch * 640, LB + L1_WO + ch * 40960 + w * 1024);
      if (FFN_TOUCH && w < 3) touch(mkd(wd, 1728 * 1280), w * 64 + lane < 160 ? lane * 128 : OOB, (xcd * 16 + r) * 20480 + w * 8192, LB + L_DUMP);
      bar0();
      dma_run<5, 128, 8192>(dwo, ovo, oso + 640 - ch * 640, LB + L1_WO + 40960 - ch * 40960 + w * 1024);
    }
    ST(6);
    v4u sm = selmask(n);
    const int kg0 = 8 * g + 4 * (n >> 3);
    int vo = kg0 * 1024 + (n & 7) * 16, so = WS_OB + (b * 32 + h) * 32768 + ch * 512 + cb * 128;
    int law = L1_LA + ((g & 2) + b) * 528 + cb * 128 + (n & 7) * 16 + (g & 1) * 8, lad = L_DUMP + lane * 8;
    PIN(law); PIN(lad);
    law = (g >> 1) == (n >> 3) ? law : lad;
    int lar = L1_LA + p * 528 + b * 256 + g * 16;
    int cxw = L1_CX + 2 * b * 144 + (32 * (cb >> 1) + 8 * q + 4 * (cb & 1) + p) * 2, cxr = L1_CX + p * 144 + g * 16;
    int fvo = g * 16384 + n * 8, fv4 = lane < 16 ? n * 8 + 4 * 16384 : OOB, fso = WS_FA + 5 * w * 16384 + (h * 4 + i) * 128;
    int wr = L1_W + b * 64 + kg0 * 2;
    int va = L1_UV + b * 16384 + (16 * q + g) * 128 + p * 8 + ((cb ^ q) << 5), to[5];
#pragma unroll
    for (int t = 0; t < 5; ++t) {
      const int T = 5 * w + t;
      to[t] = L1_WO + (T >> 2) * 8192 + rb + (((T & 3) ^ sw) << 5);
      PIN(to[t]);
    }
    int huo = lane < 8 ? WS_HU + (h * 4 + i) * 128 + lane * 16 : OOB;
    PIN(huo);
    PIN(sm); PIN(vo); PINS(so); PIN(law); PIN(lar); PIN(cxw); PIN(cxr); PIN(fvo); PIN(fv4); PINS(fso); PIN(wr); PIN(va);
    SCHED();
    DRAIN();
    ST(7);
    bar();
    v4u fv[4], fo[10];
    v4u ml;
    if (w == 7) ml = ldc(rws, lane * 512, WS_ML + h * 16);
    SCHED();
    {
#pragma unroll
      for (int k = 0; k < 4; ++k) fv[k] = cat(trd(L + va + (k >> 1) * 8192 + (k & 1) * 512), trd(L + va + (k >> 1) * 8192 + (k & 1) * 512 + 1024));
#pragma unroll
      for (int t = 0; t < 5; ++t) {
        fo[2 * t] = cat(trd(L + to[t]), trd(L + to[t] + 2048));
        fo[2 * t + 1] = cat(trd(L + to[t] + 4096), trd(L + to[t] + 6144));
      }
    }
    SCHED();
    if (w == 7) {
#pragma unroll 1
      for (u32 it = 0; it < SPIN_CAP; ++it) {
        const u64 m1 = tagbad(ml, tag);
        if (!m1) break;
        reload1(ml, m1, lane * 512, dws, WS_ML + h * 16);
      }
      ST(11);
    }
    bar0();
    v4u o0 = ldc(rws, vo, so), o1 = ldc(rws, vo + 1024, so), o2 = ldc(rws, vo + 2048, so), o3 = ldc(rws, vo + 3072, so);
    SCHED();
    if (w == 7) {
      const float m = __uint_as_float(ml.x);
      float M = fmaxf(m, dpp<XOR1>(m));
      M = fmaxf(M, dpp<XOR2>(M));
      M = fmaxf(M, dpp<ROR4>(M));
      M = fmaxf(M, dpp<ROR8>(M));
      M = x16_max(M);
      const float e = __builtin_amdgcn_exp2f(m - M);
      float Ls = e * __uint_as_float(ml.z);
      Ls += dpp<XOR1>(Ls);
      Ls += dpp<XOR2>(Ls);
      Ls += dpp<ROR4>(Ls);
      Ls += dpp<ROR8>(Ls);
      const float wn = e * gate * __builtin_amdgcn_rcpf(x16_sum(Ls));
      *(LAS u16*)(L + L1_W + lane * 2) = (u16)pk(wn, wn);
    }
    bar();
    const v2u w4 = *(LAS v2u*)(L + wr);
    v4u aw0 = sel(duplo(w4.x), sm), aw1 = sel(duphi(w4.x), sm), aw2 = sel(duplo(w4.y), sm), aw3 = sel(duphi(w4.y), sm);
    PIN(aw0); PIN(aw1); PIN(aw2); PIN(aw3);
    SCHED();
    {
      const u32 ma = max(max(max(max(o0.x, o0.y), o0.z), max(max(o0.w, o1.x), o1.y)), max(o1.z, o1.w));
      const u32 mb = max(max(max(max(o2.x, o2.y), o2.z), max(max(o2.w, o3.x), o3.y)), max(o3.z, o3.w));
      if (__builtin_expect(__builtin_amdgcn_ballot_w64(max(ma, mb) == SENT) != 0ull, 0)) {
#pragma unroll 1
        for (u32 it = 0; it < SPIN_CAP; ++it) {
          const u64 m0 = sentbad(o0), m1 = sentbad(o1), m2 = sentbad(o2), m3 = sentbad(o3);
          if (!(m0 | m1 | m2 | m3)) break;
          reload4(o0, o1, o2, o3, m0, m1, m2, m3, vo, dws, so);
        }
      }
    }
    ST(8);
    if (HEADSUP == 1 && w == 0) stc(v4u{tag, tag, tag, tag}, rws, huo);
    {
      f4 acc = mfma(aw0, o0, f4{0.f, 0.f, 0.f, 0.f});
      acc = mfma(aw1, o1, acc);
      acc = mfma(aw2, o2, acc);
      acc = mfma(aw3, o3, acc);
      *(LAS v2u*)(L + law) = v2u{pk(acc[0], acc[1]), pk(acc[2], acc[3])};
    }
    bar();
    if (HEADSUP == 2 && w == 0) stc(v4u{tag, tag, tag, tag}, rws, huo);
    {
      v4u a[4];
#pragma unroll
      for (int k = 0; k < 4; ++k) a[k] = *(LAS v4u*)(L + lar + k * 64);
      f4 d = {0.f, 0.f, 0.f, 0.f};
#pragma unroll
      for (int k = 0; k < 4; ++k) d = mfma(a[k], fv[k], d);
      const u32 c = pk(d[0] + d[2], d[1] + d[3]);
      *(LAS u16*)(L + cxw) = (u16)c;
      *(LAS u16*)(L + cxw + 144) = (u16)(c >> 16);
    }
    bar();
    ST(9);
    {
      const v4u a0 = *(LAS v4u*)(L + cxr), a1 = *(LAS v4u*)(L + cxr + 64);
      u32 cv[5];
      f4 dacc[5];
#pragma unroll
      for (int t = 0; t < 5; ++t) dacc[t] = mfma(a0, fo[2 * t], f4{0.f, 0.f, 0.f, 0.f});
#pragma unroll
      for (int t = 0; t < 5; ++t) {
        dacc[t] = mfma(a1, fo[2 * t + 1], dacc[t]);
        cv[t] = pk(dacc[t][0] + dacc[t][2], dacc[t][1] + dacc[t][3]);
      }
      const u32 c01 = (g & 1) ? cv[1] : cv[0], c23 = (g & 1) ? cv[3] : cv[2];
      stc2(v2u{(g & 2) ? c23 : c01, tag}, rws, fvo, fso);
      stc2(v2u{cv[4], tag}, rws, fv4, fso);
    }
    ST(10);
    STEND();
    __builtin_amdgcn_endpgm();
  }
  const int j = r - 16, u = xcd * 16 + j, hg = r & 1, bk = u >> 1, b = bk >> 5, kg = bk & 31;
  u32 junk;
  asm volatile("s_load_dword %0, %1, %2" : "=s"(junk) : "s"(kbase), "s"(min((j * 8 + w) * 128, CODE_BYTES - 128)) : "memory");
  if (BADCOL) {
    const int cc = (HMAP ? (xcd & 3) * 8 + (xcd & 4) : 4 * xcd) * 256 + 384;
    if (w == 0) touch(dq, lane < 40 ? lane * 8192 : OOB, 40 * j * 8192 + cc, LB + L_DUMP);
    if (w == 1) touch(mkd(wuk, 512 * 8192), lane < 32 ? lane * 8192 : OOB, 32 * j * 8192 + cc, LB + L_DUMP);
    if (w == 2) touch(mkd(wuv, 512 * 8192), lane < 32 ? lane * 8192 : OOB, 32 * j * 8192 + cc, LB + L_DUMP);
  }
  int o_vo = WS_OB + (b * 32 + 16 * hg + 4 * g + 2 * (n >> 3)) * 32768 + kg * 1024 + w * 128 + (n & 7) * 16, o_v1 = o_vo + 32768;
  PIN(o_vo);
  PIN(o_v1);
  if (CLEAN_DELAY) __builtin_amdgcn_s_sleep(CLEAN_DELAY);
  stc(v4u{SENT, SENT, SENT, SENT}, rws, o_vo);
  stc(v4u{SENT, SENT, SENT, SENT}, rws, o_v1);
  int qfw = L2_QF + 2 * w * RS + lane * 8, qfr = L2_QF + n * RS + g * 16, mxw = L2_MX + n * 80 + w * 8 + (lane >> 5) * 4, mxr = L2_MX + n * 80;
  int pfw = L2_PF + (w >> 1) * 1024 + lane * 16 + (w & 1) * 8, pfr = L2_PF + lane * 16;
  int mlo = lane < 16 ? WS_ML + bk * 512 + (16 * hg + n) * 16 : OOB;
  int vr0 = (4 * g + (n >> 2)) * RS + w * 128 + (n & 3) * 8, vr1 = vr0 + 64 * RS, kr = (16 * w + n) * RS + g * 16;
  PIN(qfw); PIN(qfr); PIN(mxw); PIN(mxr); PIN(pfw); PIN(pfr); PIN(mlo); PIN(vr0); PIN(vr1); PIN(kr);
  const int frow = 160 * (j & 3) + 20 * w + 5 * g;
  const GAS unsigned char* wp = (const GAS unsigned char*)(n < 8 ? wg : wu) + (frow + 1) * 3456 + (u < 108 ? u >> 2 : 0) * 128 + (n & 7) * 16;
  const GAS unsigned char* wp2 = wp + 3 * 3456;
  asm volatile("" : "+v"(wp), "+v"(wp2));
  SCHED();
  if (G2_DELAY) __builtin_amdgcn_s_sleep(G2_DELAY);
  ST(1);
  {
    const v4u dc = mkd(ckv, 2 * 4096 * 1024);
    const int c0 = (bk * 128 + 64 * hg + 8 * w) * 1024, c1 = (bk * 128 + 64 - 64 * hg + 8 * w) * 1024, cl = LB + 16 * w * RS;
    if (CKV_ORDER < 2) {
      dma_run<8, 1024, RS>(dc, lane * 16, c0, cl);
      if (CKV_ORDER == 0) bar0();
      dma_run<8, 1024, RS>(dc, lane * 16, c1, cl + 8 * RS);
    } else if (CKV_ORDER == 2) {
      dma_run<4, 1024, RS>(dc, lane * 16, c0, cl);
      bar0();
      dma_zip<4, 1024, RS, 4 * RS>(dc, lane * 16, c0 + 4096, c1, cl + 4 * RS);
      bar0();
      dma_run<4, 1024, RS>(dc, lane * 16, c1 + 4096, cl + 12 * RS);
    } else {
      dma_zip<8, 1024, RS, 8 * RS>(dc, lane * 16, c0, c1, cl);
    }
  }
  GET_TAG();
  asm volatile("" ::"s"(junk));
  ST(2);
  v4u kf[16], vf[16];
  {
    const int vo = lane * 16, so = WS_QA + (b * 32 + 16 * hg + 2 * w) * 2048, sb = so + 131072;
    DRAIN();
    bar0();
    ST(3);
    if (QG_DELAY) __builtin_amdgcn_s_sleep(QG_DELAY);
    v4u q0 = ldc(rws, vo, so), q1 = ldc(rws, vo + 1024, so), q2 = ldc(rws, vo + 2048, so), q3 = ldc(rws, vo + 3072, so);
    v4u q4 = ldc(rws, vo, sb), q5 = ldc(rws, vo + 1024, sb), q6 = ldc(rws, vo + 2048, sb), q7 = ldc(rws, vo + 3072, sb);
    SCHED();
#pragma unroll
    for (int s = 0; s < 16; ++s) kf[s] = *(LAS v4u*)(L + kr + s * 64);
#pragma unroll
    for (int s = 0; s < 4; ++s)
#pragma unroll
      for (int t = 0; t < 4; ++t)
        vf[s * 4 + t] = cat(trd(L + (s < 2 ? vr0 : vr1) + (s & 1) * 32 * RS + t * 32), trd(L + (s < 2 ? vr0 : vr1) + ((s & 1) * 32 + 16) * RS + t * 32));
    SCHED();
    settle4(q0, q1, q2, q3, tag, vo, dws, so);
    f4 u0 = unpk(q0), u1 = unpk(q1), u2 = unpk(q2), u3 = unpk(q3);
    PIN(u0); PIN(u1); PIN(u2); PIN(u3);
    SCHED();
    settle4(q4, q5, q6, q7, tag, vo, dws, sb);
    ST(4);
    u0 += unpk(q4);
    u1 += unpk(q5);
    u2 += unpk(q6);
    u3 += unpk(q7);
    *(LAS v2u*)(L + qfw) = v2u{pk(u0[0], u0[1]), pk(u0[2], u0[3])};
    *(LAS v2u*)(L + qfw + 512) = v2u{pk(u1[0], u1[1]), pk(u1[2], u1[3])};
    *(LAS v2u*)(L + qfw + RS) = v2u{pk(u2[0], u2[1]), pk(u2[2], u2[3])};
    *(LAS v2u*)(L + qfw + RS + 512) = v2u{pk(u3[0], u3[1]), pk(u3[2], u3[3])};
  }
  ST(5);
  bar();
  f4 sa = {0.f, 0.f, 0.f, 0.f};
  v4u wv[5];
  const v3u xd = __builtin_amdgcn_raw_buffer_load_b96(mk(x, 2560), ((n >> 3) * 1280 + frow * 2) & ~3, 0, 0);
  {
    v4u qf[16];
#pragma unroll
    for (int s = 0; s < 16; ++s) qf[s] = *(LAS v4u*)(L + qfr + s * 64);
    SCHED();
#pragma unroll
    for (int s = 0; s < 16; ++s) {
      sa = mfma(kf[s], qf[s], sa);
      if (s % 3 == 0 && s < 15) {
        wv[s / 3] = *(const GAS v4u*)((s < 9 ? wp : wp2) + ((s / 3) % 3 - 1) * 3456);
        SCHED();
      }
    }
  }
  {
    float m = fmaxf(fmaxf(sa[0], sa[1]), fmaxf(sa[2], sa[3]));
    auto s1 = __builtin_amdgcn_permlane16_swap(__float_as_uint(m), __float_as_uint(m), false, false);
    m = max2(__uint_as_float(s1[0]), __uint_as_float(s1[1]));
    *(LAS float*)(L + mxw) = m;
  }
  ST(6);
  bar();
  float M;
  {
    const f4 m0 = *(LAS f4*)(L + mxr), m1 = *(LAS f4*)(L + mxr + 16), m2 = *(LAS f4*)(L + mxr + 32), m3 = *(LAS f4*)(L + mxr + 48);
    M = max3(max3(max3(m0[0], m0[1], m0[2]), max3(m0[3], m1[0], m1[1]), max3(m1[2], m1[3], m2[0])), max3(m2[1], m2[2], m2[3]),
             max2(max3(m3[0], m3[1], m3[2]), m3[3]));
    const u32 p01 = pk(__builtin_amdgcn_exp2f(sa[0] - M), __builtin_amdgcn_exp2f(sa[1] - M));
    const u32 p23 = pk(__builtin_amdgcn_exp2f(sa[2] - M), __builtin_amdgcn_exp2f(sa[3] - M));
    *(LAS v2u*)(L + pfw) = v2u{p01, p23};
  }
  bar();
  ST(7);
  {
    v4u pf[4];
#pragma unroll
    for (int s = 0; s < 4; ++s) pf[s] = *(LAS v4u*)(L + pfr + s * 1024);
    SCHED();
    if (w == 0) {
      const v4u one = {0x3F803F80u, 0x3F803F80u, 0x3F803F80u, 0x3F803F80u};
      f4 l = {0.f, 0.f, 0.f, 0.f};
#pragma unroll
      for (int s = 0; s < 4; ++s) l = mfma(one, pf[s], l);
      stc(v4u{__float_as_uint(M), tag, __float_as_uint(l[0]), tag}, rws, mlo);
    }
    SCHED();
    f4 o[4];
#pragma unroll
    for (int t = 0; t < 4; ++t) o[t] = f4{0.f, 0.f, 0.f, 0.f};
#pragma unroll
    for (int s = 0; s < 4; ++s)
#pragma unroll
      for (int t = 0; t < 4; ++t) o[t] = mfma(pf[s], vf[s * 4 + t], o[t]);
    u32 pa[4], pb[4];
#pragma unroll
    for (int k = 0; k < 4; ++k) {
      pa[k] = pk(o[0][k], o[1][k]);
      pb[k] = pk(o[2][k], o[3][k]);
    }
#pragma unroll
    for (int k = 0; k < 2; ++k) {
      v4u pc;
      pc.x = __builtin_amdgcn_update_dpp(pa[k], pa[2 + k], ROR8, 0xf, 0xc, false);
      pc.y = __builtin_amdgcn_update_dpp(pb[k], pb[2 + k], ROR8, 0xf, 0xc, false);
      pc.z = __builtin_amdgcn_update_dpp(pa[2 + k], pa[k], ROR8, 0xf, 0x3, false);
      pc.w = __builtin_amdgcn_update_dpp(pb[2 + k], pb[k], ROR8, 0xf, 0x3, false);
      stc(pc, rws, k ? o_v1 : o_vo);
    }
  }
  ST(8);
  if (u < 108) {
    bar0();
    v4u dv[4];
    {
      const rsrc_t rd = mk(wd, 1728 * 1280);
      const int so = w < 5 ? u * 20480 + w * 256 : OOB;
#pragma unroll
      for (int t = 0; t < 4; ++t) dv[t] = __builtin_amdgcn_raw_buffer_load_b128(rd, g * 4 * 1280 + n * 16 + t * 1280, so, 0);
      SCHED();
    }
    const v4u sm = selmask(n);
    const u32 sh = (g & 1) << 4;
    const u32 a0 = __builtin_amdgcn_alignbit(xd.y, xd.x, sh), a1 = __builtin_amdgcn_alignbit(xd.z, xd.y, sh), a2 = xd.z >> sh;
    f4 acc = mfma(sel(duplo(a0), sm), wv[0], f4{0.f, 0.f, 0.f, 0.f});
    acc = mfma(sel(duphi(a0), sm), wv[1], acc);
    acc = mfma(sel(duplo(a1), sm), wv[2], acc);
    acc = mfma(sel(duphi(a1), sm), wv[3], acc);
    acc = mfma(sel(duplo(a2), sm), wv[4], acc);
    *(LAS f4*)(L + L2_QF + w * 1024 + lane * 16) = acc;
    bar();
    if (w == 6) *(LAS float*)(L + L2_MX + (lane >> 5) * 4) = 1.f - mix_gate(x, gw, lane);
    if (w == 7) {
      f4 e[8];
#pragma unroll
      for (int k = 0; k < 8; ++k) e[k] = *(LAS f4*)(L + L2_QF + k * 1024 + lane * 16);
      SCHED();
      f4 s = ((e[0] + e[1]) + (e[2] + e[3])) + ((e[4] + e[5]) + (e[6] + e[7]));
      ST(9);
      s = exchange4(s, rws, dws, u, lane, tag);
      float hv[4];
#pragma unroll
      for (int k = 0; k < 4; ++k) hv[k] = s[k] * dpp<ROR8>(s[k]) * __builtin_amdgcn_rcpf(1.f + __expf(-s[k]));
      *(LAS v2u*)(L + L2_H + (g >> 1) * 256 + n * 16 + (g & 1) * 8) = v2u{pk(hv[0], hv[1]), pk(hv[2], hv[3])};
      ST(10);
    }
    int hr = L2_H + (n >> 3) * 256 + (j & 3) * 32 + g * 8, fo = WS_FF + u * 5120 + w * 512 + (g >> 1) * 2560 + n * 32 + (g & 1) * 16;
    PIN(hr); PIN(fo);
    bar();
    if (w < 5) {
      const v2u hh = *(LAS v2u*)(L + hr);
      const float og = *(LAS float*)(L + L2_MX + (lane >> 5) * 4);
      f4 d = mfma(sel(duplo(hh.x), sm), dv[0], f4{0.f, 0.f, 0.f, 0.f});
      d = mfma(sel(duphi(hh.x), sm), dv[1], d);
      d = mfma(sel(duplo(hh.y), sm), dv[2], d);
      d = mfma(sel(duphi(hh.y), sm), dv[3], d);
      d *= og;
      stc(v4u{pk(d[0], d[1]), tag, pk(d[2], d[3]), tag}, rws, fo);
    }
    ST(11);
  }
  if (u >= 88) {
    const int T = u - 88;
    int v0, v3, st, so;
    if (w < 4) {
      so = WS_FA + T * 16384 + w * 4096;
      v0 = lane * 16;
      st = 1024;
      v3 = v0 + 3072;
    } else {
      so = WS_FF + T * 64 + (w - 4) * 27 * 5120;
      v0 = (lane >> 3) * 5120 + ((lane >> 2) & 1) * 2560 + (lane & 3) * 16;
      st = 8 * 5120;
      v3 = lane < 24 ? v0 + 24 * 5120 : v0;
    }
    int v1 = v0 + st, v2 = v1 + st;
    int fw = L2_FR + w * 128 + (lane & 7) * 16;
    int fr = L2_FR + n * 8 + (g & 1) * 4, fr2 = L2_FR + 512 + (g & 1) * 64 + n * 4;
    PIN(fr); PIN(fr2);
    fr = (lane & 32) ? fr2 : fr;
    int ovo = ((lane & 32) << 25) | ((g & 1) * 1280 + n * 2 + T * 32);
    u32 keep = (w >= 4 && lane >= 24) ? 0u : ~0u;
    const u32 o1 = 0x3F80u << ((n & 1) << 4);
    v4u pick = {(n & 2) ? 0u : o1, 0u, (n & 2) ? o1 : 0u, 0u};
    PIN(v0); PIN(v1); PIN(v2); PIN(v3); PIN(fw); PIN(fr); PIN(ovo); PIN(pick); PIN(keep);
    const rsrc_t ro = mk(out, 2560);
    if (FA_REP) {
      const int rso = HEADSUP && w < 4 ? WS_HU + (32 * w + (T & 31)) * 128 : so;
      v4u a = ldc(rws, 0, rso);
#pragma unroll 1
      for (u32 it = 0; it < SPIN_CAP; ++it) {
        if (!tagbad(a, tag)) break;
        reload1(a, ~0ull, 0, dws, rso);
      }
    }
    ST(12);
    asm volatile("" ::: "memory");
    v4u a0 = ldc(rws, v0, so), a1 = ldc(rws, v1, so), a2 = ldc(rws, v2, so), a3 = ldc(rws, v3, so);
    if (__builtin_expect(!alltag(tag, a0, a1, a2, a3), 0)) {
#pragma unroll 1
      for (u32 it = 0; it < SPIN_CAP; ++it) {
        const u64 m0 = tagbad(a0, tag), m1 = tagbad(a1, tag), m2 = tagbad(a2, tag), m3 = tagbad(a3, tag);
        if (!(m0 | m1 | m2 | m3)) break;
        reload4v(a0, a1, a2, a3, m0, m1, m2, m3, v0, v1, v2, v3, dws, so);
      }
    }
    ST(13);
    a3.x &= keep;
    a3.z &= keep;
    f4 v = mfma(pick, a0, f4{0.f, 0.f, 0.f, 0.f});
    v = mfma(pick, a1, v);
    v = mfma(pick, a2, v);
    v = mfma(pick, a3, v);
#pragma unroll
    for (int k = 0; k < 4; ++k) v[k] += dpp<ROR8>(v[k]);
    *(LAS f4*)(L + fw) = v;
    bar();
    if (w == 0) {
      float A = (*(LAS float*)(L + fr) + *(LAS float*)(L + fr + 128)) + (*(LAS float*)(L + fr + 256) + *(LAS float*)(L + fr + 384));
      auto t = __builtin_amdgcn_permlane32_swap(__float_as_uint(A), __float_as_uint(A), false, false);
      A = __uint_as_float(t[0]) + __uint_as_float(t[1]);
      __builtin_amdgcn_raw_buffer_store_b16((u16)pk(A, A), ro, ovo, 0, 0);
      if (T == 0) stc1(epoch, rws, lane == 0 ? WS_EPOCH : OOB, 0);
    }
    ST(14);
  }
  STEND();
}

extern "C" void kernel_launch(void* const* d_in, const int* in_sizes, int n_in, void* d_out, int out_size, void* d_ws, size_t ws_size,
                              hipStream_t stream) {
  mla_ffn_mix<<<256, 512, 0, stream>>>((const u16*)d_in[0], (const u16*)d_in[1], (const u16*)d_in[2], (const u16*)d_in[3],
                                       (const u16*)d_in[4], (const u16*)d_in[5], (const u16*)d_in[7], (const u16*)d_in[8],
                                       (const u16*)d_in[9], (const u16*)d_in[10], (u16*)d_out, (unsigned char*)d_ws);
}
```

```cpp
#define STAMPS
#define STMASK 33
#define PROBE_BID 5
#define PROBE_W 0
#define PROBE_A 0
#define PROBE_B 5
#include <hip/hip_runtime.h>
#include <stdint.h>

typedef unsigned u32;
typedef unsigned long long u64;
typedef unsigned short u16;
typedef u32 v4u __attribute__((ext_vector_type(4)));
typedef u32 v3u __attribute__((ext_vector_type(3)));
typedef u32 v2u __attribute__((ext_vector_type(2)));
typedef float f4 __attribute__((ext_vector_type(4)));
typedef __bf16 bf8 __attribute__((ext_vector_type(8)));
typedef __bf16 bf2 __attribute__((ext_vector_type(2)));
typedef float f2 __attribute__((ext_vector_type(2)));
typedef short s4 __attribute__((ext_vector_type(4)));
typedef short s2 __attribute__((ext_vector_type(2)));
#define GAS __attribute__((address_space(1)))
#define LAS __attribute__((address_space(3)))
typedef __amdgpu_buffer_rsrc_t rsrc_t;

constexpr int AUX_SC1 = 16;
#define CODE_BYTES 13056
#define G2_DELAY 32
#define G1_DELAY 20
#define QG_DELAY 0
#define FA_REP 1
#define BADCOL 1
#define PROBE_THR 130
#define UK_LAST 1
#define HMAP 1
#define CLEAN_DELAY 0
#define CKV_ORDER 0
#define QA_TOUCH 1
#define FFN_TOUCH 1
#define HEADSUP 0
#define PACE 63
constexpr u32 SPIN_CAP = 1u << 16;
constexpr u32 SENT = 0xFFFFFFFFu;
constexpr int OOB = 0x40000000;

constexpr int WS_EPOCH = 0;
constexpr int WS_ML = 1024;
constexpr int WS_HU = 40960;
constexpr int WS_QP = 65536;
constexpr int WS_QA = WS_QP + 128 * 1024;
constexpr int WS_FF = WS_QA + 256 * 1024;
constexpr int WS_FA = WS_FF + 108 * 5120;
constexpr int WS_OB = 2 * 1024 * 1024;
constexpr int WS_END = WS_OB + 2 * 1024 * 1024;
static_assert(WS_FA + 40 * 128 * 128 <= WS_OB, "");

constexpr int RS = 1056;
constexpr int L2_QF = 128 * RS;
constexpr int L2_PF = L2_QF + 16 * RS;
constexpr int L2_MX = L2_PF + 4096;
constexpr int L2_FR = L2_MX + 1280;
constexpr int L2_H = L2_FR + 1024;
constexpr int L_DUMP = L2_H + 512;
constexpr int LDS_BYTES = L_DUMP + 512;
constexpr int L1_UV = 0, L1_WO = 32768, L1_X = L1_WO + 81920, L1_LA = L1_X + 8192, L1_CX = L1_LA + 4 * 528, L1_Q = L1_CX + 4 * 144,
              L1_W = L1_Q + 16 * 144, L1_H = L1_W + 128;
static_assert(L1_H + 512 <= L_DUMP && LDS_BYTES <= 163840, "");

__device__ __forceinline__ f4 mfma(v4u a, v4u b, f4 c) {
  return __builtin_amdgcn_mfma_f32_16x16x32_bf16(__builtin_bit_cast(bf8, a), __builtin_bit_cast(bf8, b), c, 0, 0, 0);
}
__device__ __forceinline__ f4 mfma16(v2u a, v2u b, f4 c) {
  return __builtin_amdgcn_mfma_f32_16x16x16bf16_1k(__builtin_bit_cast(s4, a), __builtin_bit_cast(s4, b), c, 0, 0, 0);
}
__device__ __forceinline__ u32 pk(float lo, float hi) {
  f2 v = {lo, hi};
  return __builtin_bit_cast(u32, __builtin_convertvector(v, bf2));
}
__device__ __forceinline__ float blo(u32 v) { return __uint_as_float(v << 16); }
__device__ __forceinline__ float bhi(u32 v) { return __uint_as_float(v & 0xFFFF0000u); }
template <int CTRL> __device__ __forceinline__ float dpp(float x) {
  return __builtin_bit_cast(float, __builtin_amdgcn_mov_dpp(__builtin_bit_cast(int, x), CTRL, 0xf, 0xf, true));
}
constexpr int XOR1 = 0xB1, XOR2 = 0x4E, ROR8 = 0x128, ROR4 = 0x124;
__device__ __forceinline__ float max3(float a, float b, float c) {
  float r;
  asm("v_max3_f32 %0, %1, %2, %3" : "=v"(r) : "v"(a), "v"(b), "v"(c));
  return r;
}
__device__ __forceinline__ float max2(float a, float b) {
  float r;
  asm("v_max_f32 %0, %1, %2" : "=v"(r) : "v"(a), "v"(b));
  return r;
}
__device__ __forceinline__ float x16_max(float x) {
  auto s = __builtin_amdgcn_permlane16_swap(__float_as_uint(x), __float_as_uint(x), false, false);
  return fmaxf(__uint_as_float(s[0]), __uint_as_float(s[1]));
}
__device__ __forceinline__ float x16_sum(float x) {
  auto s = __builtin_amdgcn_permlane16_swap(__float_as_uint(x), __float_as_uint(x), false, false);
  return __uint_as_float(s[0]) + __uint_as_float(s[1]);
}
__device__ __forceinline__ float x32_max(float x) {
  auto t = __builtin_amdgcn_permlane32_swap(__float_as_uint(x), __float_as_uint(x), false, false);
  return fmaxf(__uint_as_float(t[0]), __uint_as_float(t[1]));
}
__device__ __forceinline__ rsrc_t mk(const void* p, u32 bytes) { return __builtin_amdgcn_make_buffer_rsrc((void*)p, 0, bytes, 0x00020000); }
__device__ __forceinline__ v4u mkd(const void* p, u32 bytes) {
  return v4u{(u32)(size_t)p, (u32)((size_t)p >> 32) & 0xFFFFu, bytes, 0x00020000u};
}
__device__ __forceinline__ v4u ldc(rsrc_t r, int vo, int so) { return __builtin_amdgcn_raw_buffer_load_b128(r, vo, so, AUX_SC1); }
__device__ __forceinline__ void stc(v4u v, rsrc_t r, int vo) { __builtin_amdgcn_raw_buffer_store_b128(v, r, vo, 0, AUX_SC1); }
__device__ __forceinline__ void stc2(v2u v, rsrc_t r, int vo, int so) { __builtin_amdgcn_raw_buffer_store_b64(v, r, vo, so, AUX_SC1); }
__device__ __forceinline__ void stc1(u32 v, rsrc_t r, int vo, int so) { __builtin_amdgcn_raw_buffer_store_b32(v, r, vo, so, AUX_SC1); }
template <int N, int SSTEP, int LSTEP, int KEEP = 63> __device__ __forceinline__ void dma_run(v4u d, int vo, int so, int lds) {
  int t;
  asm volatile("s_mov_b32 m0, %[l]\n\ts_mov_b32 %[t], %[s]\n\t.rept %[n]\n\tbuffer_load_dwordx4 %[v], %[d], %[t] offen lds\n\t"
               "s_add_u32 m0, m0, %[ls]\n\ts_add_u32 %[t], %[t], %[ss]\n\t.if %[k] < 63\n\ts_waitcnt vmcnt(%[k])\n\t.endif\n\t.endr"
               : [t] "=&s"(t)
               : [v] "v"(vo), [d] "s"(d), [s] "s"(so), [l] "s"(lds), [n] "i"(N), [ls] "i"(LSTEP), [ss] "i"(SSTEP), [k] "i"(KEEP)
               : "memory", "scc");
}
template <int N, int SSTEP, int LSTEP> __device__ __forceinline__ void dma_run2(v4u d, int va, int vb, int so, int lds) {
  int t;
  asm volatile("s_mov_b32 m0, %[l]\n\ts_mov_b32 %[t], %[s]\n\t.rept %[n]\n\tbuffer_load_dwordx4 %[va], %[d], %[t] offen lds\n\t"
               "s_add_u32 m0, m0, 0x400\n\ts_nop 0\n\tbuffer_load_dwordx4 %[vb], %[d], %[t] offen lds\n\t"
               "s_add_u32 m0, m0, %[ls]\n\ts_add_u32 %[t], %[t], %[ss]\n\t.endr"
               : [t] "=&s"(t)
               : [va] "v"(va), [vb] "v"(vb), [d] "s"(d), [s] "s"(so), [l] "s"(lds), [n] "i"(N), [ls] "i"(LSTEP - 1024), [ss] "i"(SSTEP)
               : "memory", "scc");
}
template <int N, int SSTEP, int LSTEP, int LD> __device__ __forceinline__ void dma_zip(v4u d, int vo, int sa, int sb, int lds) {
  int t, t2;
  asm volatile("s_mov_b32 m0, %[l]\n\ts_mov_b32 %[t], %[sa]\n\ts_mov_b32 %[u], %[sb]\n\t.rept %[n]\n\t"
               "buffer_load_dwordx4 %[v], %[d], %[t] offen lds\n\ts_add_u32 m0, m0, %[ld]\n\ts_add_u32 %[t], %[t], %[ss]\n\t"
               "buffer_load_dwordx4 %[v], %[d], %[u] offen lds\n\ts_sub_u32 m0, m0, %[lb]\n\ts_add_u32 %[u], %[u], %[ss]\n\t.endr"
               : [t] "=&s"(t), [u] "=&s"(t2)
               : [v] "v"(vo), [d] "s"(d), [sa] "s"(sa), [sb] "s"(sb), [l] "s"(lds), [n] "i"(N), [ld] "i"(LD), [lb] "i"(LD - LSTEP), [ss] "i"(SSTEP)
               : "memory", "scc");
}
__device__ __forceinline__ void touch(v4u d, int vo, int so, int lds) {
  asm volatile("s_mov_b32 m0, %[l]\n\ts_nop 0\n\tbuffer_load_dword %[v], %[d], %[s] offen lds" ::[v] "v"(vo), [d] "s"(d), [s] "s"(so), [l] "s"(lds)
               : "memory");
}
__device__ __forceinline__ v2u trd(LAS unsigned char* p) {
  return __builtin_bit_cast(v2u, __builtin_amdgcn_ds_read_tr16_b64_v4i16((LAS s4*)p));
}
__device__ __forceinline__ v4u cat(v2u a, v2u b) { return v4u{a.x, a.y, b.x, b.y}; }
#define SCHED() __builtin_amdgcn_sched_barrier(0)
#define PIN(x) asm volatile("" : "+v"(x))
#define PINS(x) asm volatile("" : "+s"(x))
__device__ __forceinline__ void bar() {
  asm volatile("s_waitcnt lgkmcnt(0)" ::: "memory");
  __builtin_amdgcn_s_barrier();
  asm volatile("" ::: "memory");
}
__device__ __forceinline__ void bar0() {
  SCHED();
  asm volatile("" ::: "memory");
  __builtin_amdgcn_s_barrier();
  asm volatile("" ::: "memory");
  SCHED();
}
#define VMW(n) asm volatile("s_waitcnt vmcnt(" #n ")" ::: "memory")
#define DRAIN() VMW(0)
__device__ __forceinline__ u64 tagbad(v4u a, u32 tag) { return __builtin_amdgcn_ballot_w64(a.y != tag) | __builtin_amdgcn_ballot_w64(a.w != tag); }
__device__ __forceinline__ u64 sentbad(v4u a) { return __builtin_amdgcn_ballot_w64(max(max(a.x, a.y), max(a.z, a.w)) == SENT); }
#define CX(i) "v_cmpx_eq_u32 vcc, %[t], %[a" #i "]\n\t"
__device__ __forceinline__ bool alltag(u32 tag, v4u p, v4u q, v4u r, v4u s) {
  u64 e;
  asm volatile(CX(0) CX(1) CX(2) CX(3) CX(4) CX(5) CX(6) CX(7) "s_mov_b64 %[e], exec\n\ts_mov_b64 exec, -1\n\ts_nop 1"
               : [e] "=s"(e)
               : [t] "s"(tag), [a0] "v"(p.y), [a1] "v"(p.w), [a2] "v"(q.y), [a3] "v"(q.w), [a4] "v"(r.y), [a5] "v"(r.w), [a6] "v"(s.y), [a7] "v"(s.w)
               : "vcc");
  return e == ~0ull;
}
#define RL1(i) "s_mov_b64 exec, %[m" #i "]\n\ts_cbranch_execz " #i "f\n\tbuffer_load_dwordx4 %[a" #i "], %[v" #i "], %[rs], %[so] offen offset:%[o" #i "] sc1\n" #i ":\n\t"
__device__ __forceinline__ void reload4(v4u& a1, v4u& a2, v4u& a3, v4u& a4, u64 m1, u64 m2, u64 m3, u64 m4, int v, v4u rs, int so) {
  asm volatile("s_nop 4\n\t" RL1(1) RL1(2) RL1(3) RL1(4) "s_mov_b64 exec, -1\n\ts_waitcnt vmcnt(0)"
               : [a1] "+v"(a1), [a2] "+v"(a2), [a3] "+v"(a3), [a4] "+v"(a4)
               : [m1] "s"(m1), [m2] "s"(m2), [m3] "s"(m3), [m4] "s"(m4), [v1] "v"(v), [v2] "v"(v), [v3] "v"(v), [v4] "v"(v), [rs] "s"(rs),
                 [so] "s"(so), [o1] "i"(0), [o2] "i"(1024), [o3] "i"(2048), [o4] "i"(3072)
               : "memory");
}
__device__ __forceinline__ void reload3(v4u& a1, v4u& a2, v4u& a3, u64 m1, u64 m2, u64 m3, int v1, int v2, int v3, v4u rs, int so) {
  asm volatile("s_nop 4\n\t" RL1(1) RL1(2) RL1(3) "s_mov_b64 exec, -1\n\ts_waitcnt vmcnt(0)"
               : [a1] "+v"(a1), [a2] "+v"(a2), [a3] "+v"(a3)
               : [m1] "s"(m1), [m2] "s"(m2), [m3] "s"(m3), [v1] "v"(v1), [v2] "v"(v2), [v3] "v"(v3), [rs] "s"(rs), [so] "s"(so), [o1] "i"(0),
                 [o2] "i"(0), [o3] "i"(0)
               : "memory");
}
__device__ __forceinline__ void reload1(v4u& a1, u64 m1, int v1, v4u rs, int so) {
  asm volatile("s_nop 4\n\t" RL1(1) "s_mov_b64 exec, -1\n\ts_waitcnt vmcnt(0)"
               : [a1] "+v"(a1)
               : [m1] "s"(m1), [v1] "v"(v1), [rs] "s"(rs), [so] "s"(so), [o1] "i"(0)
               : "memory");
}
__device__ __forceinline__ void reload4v(v4u& a1, v4u& a2, v4u& a3, v4u& a4, u64 m1, u64 m2, u64 m3, u64 m4, int v1, int v2, int v3, int v4, v4u rs,
                                         int so) {
  asm volatile("s_nop 4\n\t" RL1(1) RL1(2) RL1(3) RL1(4) "s_mov_b64 exec, -1\n\ts_waitcnt vmcnt(0)"
               : [a1] "+v"(a1), [a2] "+v"(a2), [a3] "+v"(a3), [a4] "+v"(a4)
               : [m1] "s"(m1), [m2] "s"(m2), [m3] "s"(m3), [m4] "s"(m4), [v1] "v"(v1), [v2] "v"(v2), [v3] "v"(v3), [v4] "v"(v4), [rs] "s"(rs),
                 [so] "s"(so), [o1] "i"(0), [o2] "i"(0), [o3] "i"(0), [o4] "i"(0)
               : "memory");
}
__device__ __forceinline__ void settle4(v4u& a, v4u& b, v4u& c, v4u& d, u32 tag, int v, v4u rs, int so) {
  if (__builtin_expect(!alltag(tag, a, b, c, d), 0)) {
#pragma unroll 1
    for (u32 it = 0; it < SPIN_CAP; ++it) {
      const u64 m0 = tagbad(a, tag), m1 = tagbad(b, tag), m2 = tagbad(c, tag), m3 = tagbad(d, tag);
      if (!(m0 | m1 | m2 | m3)) break;
      __builtin_amdgcn_s_sleep(2);
      reload4(a, b, c, d, m0, m1, m2, m3, v, rs, so);
    }
  }
}
__device__ __forceinline__ v4u sel(u32 dup, v4u m) { return v4u{dup & m.x, dup & m.y, dup & m.z, dup & m.w}; }
__device__ __forceinline__ v4u selmask(int n) {
  const u32 v = 0xFFFFu << ((n & 1) << 4);
  const int q = (n >> 1) & 3;
  return v4u{q == 0 ? v : 0u, q == 1 ? v : 0u, q == 2 ? v : 0u, q == 3 ? v : 0u};
}
__device__ __forceinline__ u32 duplo(u32 d) { return __builtin_amdgcn_perm(d, d, 0x01000100u); }
__device__ __forceinline__ u32 duphi(u32 d) { return __builtin_amdgcn_perm(d, d, 0x03020302u); }
__device__ __forceinline__ f4 unpk(v4u a) { return f4{blo(a.x), bhi(a.x), blo(a.z), bhi(a.z)}; }

__device__ __forceinline__ f4 exchange4(f4 s, rsrc_t rws, v4u dws, int slot, int lane, u32 tag) {
  stc(v4u{pk(s[0], s[1]), tag, pk(s[2], s[3]), tag}, rws, WS_QP + slot * 1024 + lane * 16);
  const int v0 = (slot & 3) * 1024 + lane * 16, so = WS_QP + (slot & ~3) * 1024;
  DRAIN();
  v4u q1 = ldc(rws, v0 ^ 1024, so), q2 = ldc(rws, v0 ^ 2048, so), q3 = ldc(rws, v0 ^ 3072, so);
#pragma unroll 1
  for (u32 it = 0; it < SPIN_CAP; ++it) {
    const u64 m1 = tagbad(q1, tag), m2 = tagbad(q2, tag), m3 = tagbad(q3, tag);
    if (!(m1 | m2 | m3)) break;
    __builtin_amdgcn_s_sleep(2);
    reload3(q1, q2, q3, m1, m2, m3, v0 ^ 1024, v0 ^ 2048, v0 ^ 3072, dws, so);
  }
  return (s + unpk(q1)) + (unpk(q2) + unpk(q3));
}

__device__ __forceinline__ float mix_gate(const u16* x, const u16* gw, int lane) {
  const rsrc_t rx = mk(x, 2560), rg = mk(gw, 1280);
  v2u a[5], c[5];
#pragma unroll
  for (int k = 0; k < 5; ++k) {
    a[k] = __builtin_amdgcn_raw_buffer_load_b64(rx, (lane >> 5) * 1280 + (lane & 31) * 8 + k * 256, 0, 0);
    c[k] = __builtin_amdgcn_raw_buffer_load_b64(rg, (lane & 31) * 8 + k * 256, 0, 0);
  }
  float s = 0.f;
#pragma unroll
  for (int k = 0; k < 5; ++k) s += (blo(a[k].x) * blo(c[k].x) + bhi(a[k].x) * bhi(c[k].x)) + (blo(a[k].y) * blo(c[k].y) + bhi(a[k].y) * bhi(c[k].y));
  s += dpp<XOR1>(s);
  s += dpp<XOR2>(s);
  s += dpp<ROR4>(s);
  s += dpp<ROR8>(s);
  return 1.f / (1.f + __expf(-x16_sum(s)));
}

#define NST 16
#define ST(i) do { if ((STMASK >> (i)) & 1) { SCHED(); asm volatile("s_memrealtime s[98:99]\n s_waitcnt lgkmcnt(0)\n v_writelane_b32 %0, s98, " #i : "+v"(st_) :: "s98", "s99"); SCHED(); } } while (0)
#define STEND() do { if (bid == PROBE_BID && w == PROBE_W) { const u64 e0 = __builtin_amdgcn_s_memrealtime(); \
    u32 d = (__builtin_amdgcn_readlane(st_, PROBE_B) - __builtin_amdgcn_readlane(st_, PROBE_A)) * 10u; d = d < 100000u ? d : 100000u; \
    while (__builtin_amdgcn_s_memrealtime() < e0 + d) __builtin_amdgcn_s_sleep(2); } } while (0)

extern "C" __global__ void __launch_bounds__(512, 2)
mla_ffn_mix(const u16* __restrict__ x, const u16* __restrict__ ckv, const u16* __restrict__ wq, const u16* __restrict__ wuk,
            const u16* __restrict__ wuv, const u16* __restrict__ wo, const u16* __restrict__ wg, const u16* __restrict__ wu,
            const u16* __restrict__ wd, const u16* __restrict__ gw, u16* __restrict__ out, unsigned char* __restrict__ ws) {
  __shared__ __attribute__((aligned(16))) unsigned char smem[LDS_BYTES];
  LAS unsigned char* L = (LAS unsigned char*)smem;
  const int LB = (int)(u32)(size_t)L;
  u32 st_ = 0;
  ST(0);
  const int tid = threadIdx.x, bid = blockIdx.x;
  u64 kbase;
  asm volatile("s_getpc_b64 %0\n\ts_andn2_b64 %0, %0, 0xff" : "=s"(kbase)::"scc");
  const int lane = tid & 63, w = __builtin_amdgcn_readfirstlane(tid >> 6);
  const int xcd = bid & 7, r = bid >> 3;
  const int n = lane & 15, g = lane >> 4;
  const int h = (HMAP ? (xcd & 3) * 8 + (xcd & 4) : 4 * xcd) + (r >> 2), i = r & 3, dh = i >> 1, ch = i & 1;
  const int lr = lane >> 3, ckx = ((lane & 7) ^ (lr & 6)) << 4;
  const int col = h * 256 + dh * 128, r0 = 80 * (w ^ (4 * ch));
  int qva = ((lr >> 2) * 20 + (lr & 3)) * 8192 + ckx, qvb = qva + 40 * 8192, qso = r0 * 8192 + col, qld = LB + L1_WO + w * 10240;
  PIN(qva); PIN(qvb); PINS(qso); PINS(qld);
  SCHED();
  asm volatile("" ::"s"(x), "s"(ckv), "s"(wq), "s"(wuk), "s"(wuv), "s"(wo), "s"(wg), "s"(wu), "s"(wd), "s"(gw), "s"(out), "s"(ws));
  const v4u dq = mkd(wq, 640 * 8192);
  if (bid < 128 && w < 4) dma_run2<5, 4 * 8192, 2048>(dq, qva, qvb, qso, qld);
  SCHED();
  u32 epoch, tag;
  asm volatile("s_load_dword %0, %1, 0x0" : "=s"(epoch) : "s"(ws) : "memory");
#define GET_TAG()                                                                          \
  do {                                                                                     \
    asm volatile("s_waitcnt lgkmcnt(0)" : "+s"(epoch)::"memory");                          \
    epoch += 1u;                                                                           \
    tag = (epoch & 0x3FFFu) | ((epoch << 1) & 0x3FFF8000u) | ((epoch << 2) & 0x80000000u); \
    PINS(tag);                                                                             \
  } while (0)
  const rsrc_t rws = mk(ws, WS_END);
  const v4u dws = mkd(ws, WS_END);

  if (__builtin_expect(r < 16, 1)) {
    const int q = n >> 2, p = n & 3, sw = 2 * (g & 1) + (q >> 1), rb = (4 * g + q) * 128 + p * 8;
    {
      const v4u dk = mkd(wuk, 512 * 8192);
      int ta[4];
#pragma unroll
      for (int t = 0; t < 4; ++t) {
        ta[t] = L1_WO + w * 10240 + rb + ((t ^ sw) << 5);
        PIN(ta[t]);
      }
      int kvo = lr * 8192 + ckx, xvo = (lane >> 5) * 1280 + (lane & 31) * 16;
      PIN(kvo); PIN(xvo);
      bar0();
      if (w >= 4) dma_run2<5, 4 * 8192, 2048>(dq, qva, qvb, qso, qld);
      dma_run<1, 0, 0>(mkd(x, 2560), xvo, r0 * 2, LB + L1_X + w * 1024);
      GET_TAG();
      int qw = L1_Q + (8 * (g >> 1) + w) * 144 + (g & 1) * 64 + n * 2, qr = L1_Q + n * 144 + g * 16;
      int svo = ((lane & 16) << 26) | ((g >> 1) * 65536 + n * 8), sso = WS_QA + dh * 131072 + h * 2048 + ch * 1024 + w * 128;
      int xr = L1_X + w * 1024 + (n >> 3) * 512 + g * 40, ur0 = L1_UV + w * 4096 + n * 256 + ((g ^ (p << 1)) << 4), ur1 = ur0 ^ 64;
      PIN(qw); PIN(qr); PIN(svo); PINS(sso); PIN(xr); PIN(ur0); PIN(ur1);
      if (UK_LAST) bar0();
      dma_run<4, 8 * 8192, 1024>(dk, kvo, (256 * ch + 32 * w) * 8192 + col, LB + L1_UV + w * 4096);
      ST(1);
      VMW(4);
      v2u xf[5], f[20];
#pragma unroll
      for (int a = 0; a < 5; ++a) {
        xf[a] = *(LAS v2u*)(L + xr + a * 8);
#pragma unroll
        for (int t = 0; t < 4; ++t) f[a * 4 + t] = trd(L + ta[t] + a * 2048);
      }
      SCHED();
      f4 d[4];
#pragma unroll
      for (int t = 0; t < 4; ++t) d[t] = f4{0.f, 0.f, 0.f, 0.f};
#pragma unroll
      for (int a = 0; a < 5; ++a)
#pragma unroll
        for (int t = 0; t < 4; ++t) d[t] = mfma16(xf[a], f[a * 4 + t], d[t]);
      const u32 qq = pk((g & 1) ? d[2][0] : d[0][0], (g & 1) ? d[3][0] : d[1][0]);
      *(LAS u16*)(L + qw) = (u16)qq;
      *(LAS u16*)(L + qw + 32) = (u16)(qq >> 16);
      ST(2);
      SCHED();
      VMW(0);
      v4u uk[4];
#pragma unroll
      for (int k = 0; k < 4; ++k) uk[k] = *(LAS v4u*)(L + ((k & 1) ? ur1 : ur0) + (k >> 1) * 128);
      ST(12);
      bar();
      const v4u q0 = *(LAS v4u*)(L + qr), q1 = *(LAS v4u*)(L + qr + 64);
      f4 a0 = mfma(q0, uk[0], f4{0.f, 0.f, 0.f, 0.f});
      f4 a1 = mfma(q0, uk[2], f4{0.f, 0.f, 0.f, 0.f});
      a0 = mfma(q1, uk[1], a0);
      a1 = mfma(q1, uk[3], a1);
      constexpr float SC = 0.08838834764831845f * 1.4426950408889634f;
      const float v0 = x16_sum((a0[0] + a0[1]) + (a0[2] + a0[3])) * SC, v1 = x16_sum((a1[0] + a1[1]) + (a1[2] + a1[3])) * SC;
      stc2(v2u{pk(v0, v1), tag}, rws, svo, sso);
    }
    bar0();
    ST(5);
    float gate = 0.f;
    if (w == 7) {
      gate = mix_gate(x, gw, lane);
      PIN(gate);
    } else if (G1_DELAY) {
      __builtin_amdgcn_s_sleep(G1_DELAY);
    }
    bar0();
    const int b = w >> 2, cb = w & 3;
    if (QA_TOUCH && w == 0) touch(dws, lane * 128, WS_QA + (r >> 3) * 131072 + (xcd >> 2) * 65536 + (r & 7) * 8192, LB + L_DUMP);
    if (FFN_TOUCH && xcd * 16 + r < 108) {
      const int fu = xcd * 16 + r;
      if (w >= 1 && w < 7) {
        const int k = w - 1 - (w >= 4 ? 3 : 0), rr = k * 64 + lane;
        touch(mkd(w >= 4 ? wu : wg, 640 * 3456), rr < 160 ? rr * 3456 : OOB, 160 * i * 3456 + (fu >> 2) * 128, LB + L_DUMP);
      }
    }
    {
      const int uvo = lr * 8192 + (((lane & 7) ^ (w & 6)) << 4), ovo = lr * 1280 + ckx;
      dma_run<4, 64 * 8192, 8192, PACE>(mkd(wuv, 512 * 8192), uvo, (256 * ch + 8 * w) * 8192 + col, LB + L1_UV + w * 1024);
      const v4u dwo = mkd(wo, 4096 * 1280);
      const int oso = (h * 128 + 64 * dh + 8 * w) * 1280;
      dma_run<5, 128, 8192, PACE>(dwo, ovo, oso + ch * 640, LB + L1_WO + ch * 40960 + w * 1024);
      if (FFN_TOUCH && w < 3) touch(mkd(wd, 1728 * 1280), w * 64 + lane < 160 ? lane * 128 : OOB, (xcd * 16 + r) * 20480 + w * 8192, LB + L_DUMP);
      bar0();
      dma_run<5, 128, 8192>(dwo, ovo, oso + 640 - ch * 640, LB + L1_WO + 40960 - ch * 40960 + w * 1024);
    }
    ST(6);
    v4u sm = selmask(n);
    const int kg0 = 8 * g + 4 * (n >> 3);
    int vo = kg0 * 1024 + (n & 7) * 16, so = WS_OB + (b * 32 + h) * 32768 + ch * 512 + cb * 128;
    int law = L1_LA + ((g & 2) + b) * 528 + cb * 128 + (n & 7) * 16 + (g & 1) * 8, lad = L_DUMP + lane * 8;
    PIN(law); PIN(lad);
    law = (g >> 1) == (n >> 3) ? law : lad;
    int lar = L1_LA + p * 528 + b * 256 + g * 16;
    int cxw = L1_CX + 2 * b * 144 + (32 * (cb >> 1) + 8 * q + 4 * (cb & 1) + p) * 2, cxr = L1_CX + p * 144 + g * 16;
    int fvo = g * 16384 + n * 8, fv4 = lane < 16 ? n * 8 + 4 * 16384 : OOB, fso = WS_FA + 5 * w * 16384 + (h * 4 + i) * 128;
    int wr = L1_W + b * 64 + kg0 * 2;
    int va = L1_UV + b * 16384 + (16 * q + g) * 128 + p * 8 + ((cb ^ q) << 5), to[5];
#pragma unroll
    for (int t = 0; t < 5; ++t) {
      const int T = 5 * w + t;
      to[t] = L1_WO + (T >> 2) * 8192 + rb + (((T & 3) ^ sw) << 5);
      PIN(to[t]);
    }
    int huo = lane < 8 ? WS_HU + (h * 4 + i) * 128 + lane * 16 : OOB;
    PIN(huo);
    PIN(sm); PIN(vo); PINS(so); PIN(law); PIN(lar); PIN(cxw); PIN(cxr); PIN(fvo); PIN(fv4); PINS(fso); PIN(wr); PIN(va);
    SCHED();
    DRAIN();
    ST(7);
    bar();
    v4u fv[4], fo[10];
    v4u ml;
    if (w == 7) ml = ldc(rws, lane * 512, WS_ML + h * 16);
    SCHED();
    {
#pragma unroll
      for (int k = 0; k < 4; ++k) fv[k] = cat(trd(L + va + (k >> 1) * 8192 + (k & 1) * 512), trd(L + va + (k >> 1) * 8192 + (k & 1) * 512 + 1024));
#pragma unroll
      for (int t = 0; t < 5; ++t) {
        fo[2 * t] = cat(trd(L + to[t]), trd(L + to[t] + 2048));
        fo[2 * t + 1] = cat(trd(L + to[t] + 4096), trd(L + to[t] + 6144));
      }
    }
    SCHED();
    if (w == 7) {
#pragma unroll 1
      for (u32 it = 0; it < SPIN_CAP; ++it) {
        const u64 m1 = tagbad(ml, tag);
        if (!m1) break;
        reload1(ml, m1, lane * 512, dws, WS_ML + h * 16);
      }
      ST(11);
    }
    bar0();
    v4u o0 = ldc(rws, vo, so), o1 = ldc(rws, vo + 1024, so), o2 = ldc(rws, vo + 2048, so), o3 = ldc(rws, vo + 3072, so);
    SCHED();
    if (w == 7) {
      const float m = __uint_as_float(ml.x);
      float M = fmaxf(m, dpp<XOR1>(m));
      M = fmaxf(M, dpp<XOR2>(M));
      M = fmaxf(M, dpp<ROR4>(M));
      M = fmaxf(M, dpp<ROR8>(M));
      M = x16_max(M);
      const float e = __builtin_amdgcn_exp2f(m - M);
      float Ls = e * __uint_as_float(ml.z);
      Ls += dpp<XOR1>(Ls);
      Ls += dpp<XOR2>(Ls);
      Ls += dpp<ROR4>(Ls);
      Ls += dpp<ROR8>(Ls);
      const float wn = e * gate * __builtin_amdgcn_rcpf(x16_sum(Ls));
      *(LAS u16*)(L + L1_W + lane * 2) = (u16)pk(wn, wn);
    }
    bar();
    const v2u w4 = *(LAS v2u*)(L + wr);
    v4u aw0 = sel(duplo(w4.x), sm), aw1 = sel(duphi(w4.x), sm), aw2 = sel(duplo(w4.y), sm), aw3 = sel(duphi(w4.y), sm);
    PIN(aw0); PIN(aw1); PIN(aw2); PIN(aw3);
    SCHED();
    {
      const u32 ma = max(max(max(max(o0.x, o0.y), o0.z), max(max(o0.w, o1.x), o1.y)), max(o1.z, o1.w));
      const u32 mb = max(max(max(max(o2.x, o2.y), o2.z), max(max(o2.w, o3.x), o3.y)), max(o3.z, o3.w));
      if (__builtin_expect(__builtin_amdgcn_ballot_w64(max(ma, mb) == SENT) != 0ull, 0)) {
#pragma unroll 1
        for (u32 it = 0; it < SPIN_CAP; ++it) {
          const u64 m0 = sentbad(o0), m1 = sentbad(o1), m2 = sentbad(o2), m3 = sentbad(o3);
          if (!(m0 | m1 | m2 | m3)) break;
          reload4(o0, o1, o2, o3, m0, m1, m2, m3, vo, dws, so);
        }
      }
    }
    ST(8);
    if (HEADSUP == 1 && w == 0) stc(v4u{tag, tag, tag, tag}, rws, huo);
    {
      f4 acc = mfma(aw0, o0, f4{0.f, 0.f, 0.f, 0.f});
      acc = mfma(aw1, o1, acc);
      acc = mfma(aw2, o2, acc);
      acc = mfma(aw3, o3, acc);
      *(LAS v2u*)(L + law) = v2u{pk(acc[0], acc[1]), pk(acc[2], acc[3])};
    }
    bar();
    if (HEADSUP == 2 && w == 0) stc(v4u{tag, tag, tag, tag}, rws, huo);
    {
      v4u a[4];
#pragma unroll
      for (int k = 0; k < 4; ++k) a[k] = *(LAS v4u*)(L + lar + k * 64);
      f4 d = {0.f, 0.f, 0.f, 0.f};
#pragma unroll
      for (int k = 0; k < 4; ++k) d = mfma(a[k], fv[k], d);
      const u32 c = pk(d[0] + d[2], d[1] + d[3]);
      *(LAS u16*)(L + cxw) = (u16)c;
      *(LAS u16*)(L + cxw + 144) = (u16)(c >> 16);
    }
    bar();
    ST(9);
    {
      const v4u a0 = *(LAS v4u*)(L + cxr), a1 = *(LAS v4u*)(L + cxr + 64);
      u32 cv[5];
      f4 dacc[5];
#pragma unroll
      for (int t = 0; t < 5; ++t) dacc[t] = mfma(a0, fo[2 * t], f4{0.f, 0.f, 0.f, 0.f});
#pragma unroll
      for (int t = 0; t < 5; ++t) {
        dacc[t] = mfma(a1, fo[2 * t + 1], dacc[t]);
        cv[t] = pk(dacc[t][0] + dacc[t][2], dacc[t][1] + dacc[t][3]);
      }
      const u32 c01 = (g & 1) ? cv[1] : cv[0], c23 = (g & 1) ? cv[3] : cv[2];
      stc2(v2u{(g & 2) ? c23 : c01, tag}, rws, fvo, fso);
      stc2(v2u{cv[4], tag}, rws, fv4, fso);
    }
    ST(10);
    STEND();
    __builtin_amdgcn_endpgm();
  }
  const int j = r - 16, u = xcd * 16 + j, hg = r & 1, bk = u >> 1, b = bk >> 5, kg = bk & 31;
  u32 junk;
  asm volatile("s_load_dword %0, %1, %2" : "=s"(junk) : "s"(kbase), "s"(min((j * 8 + w) * 128, CODE_BYTES - 128)) : "memory");
  if (BADCOL) {
    const int cc = (HMAP ? (xcd & 3) * 8 + (xcd & 4) : 4 * xcd) * 256 + 384;
    if (w == 0) touch(dq, lane < 40 ? lane * 8192 : OOB, 40 * j * 8192 + cc, LB + L_DUMP);
    if (w == 1) touch(mkd(wuk, 512 * 8192), lane < 32 ? lane * 8192 : OOB, 32 * j * 8192 + cc, LB + L_DUMP);
    if (w == 2) touch(mkd(wuv, 512 * 8192), lane < 32 ? lane * 8192 : OOB, 32 * j * 8192 + cc, LB + L_DUMP);
  }
  int o_vo = WS_OB + (b * 32 + 16 * hg + 4 * g + 2 * (n >> 3)) * 32768 + kg * 1024 + w * 128 + (n & 7) * 16, o_v1 = o_vo + 32768;
  PIN(o_vo);
  PIN(o_v1);
  if (CLEAN_DELAY) __builtin_amdgcn_s_sleep(CLEAN_DELAY);
  stc(v4u{SENT, SENT, SENT, SENT}, rws, o_vo);
  stc(v4u{SENT, SENT, SENT, SENT}, rws, o_v1);
  int qfw = L2_QF + 2 * w * RS + lane * 8, qfr = L2_QF + n * RS + g * 16, mxw = L2_MX + n * 80 + w * 8 + (lane >> 5) * 4, mxr = L2_MX + n * 80;
  int pfw = L2_PF + (w >> 1) * 1024 + lane * 16 + (w & 1) * 8, pfr = L2_PF + lane * 16;
  int mlo = lane < 16 ? WS_ML + bk * 512 + (16 * hg + n) * 16 : OOB;
  int vr0 = (4 * g + (n >> 2)) * RS + w * 128 + (n & 3) * 8, vr1 = vr0 + 64 * RS, kr = (16 * w + n) * RS + g * 16;
  PIN(qfw); PIN(qfr); PIN(mxw); PIN(mxr); PIN(pfw); PIN(pfr); PIN(mlo); PIN(vr0); PIN(vr1); PIN(kr);
  const int frow = 160 * (j & 3) + 20 * w + 5 * g;
  const GAS unsigned char* wp = (const GAS unsigned char*)(n < 8 ? wg : wu) + (frow + 1) * 3456 + (u < 108 ? u >> 2 : 0) * 128 + (n & 7) * 16;
  const GAS unsigned char* wp2 = wp + 3 * 3456;
  asm volatile("" : "+v"(wp), "+v"(wp2));
  SCHED();
  if (G2_DELAY) __builtin_amdgcn_s_sleep(G2_DELAY);
  ST(1);
  {
    const v4u dc = mkd(ckv, 2 * 4096 * 1024);
    const int c0 = (bk * 128 + 64 * hg + 8 * w) * 1024, c1 = (bk * 128 + 64 - 64 * hg + 8 * w) * 1024, cl = LB + 16 * w * RS;
    if (CKV_ORDER < 2) {
      dma_run<8, 1024, RS>(dc, lane * 16, c0, cl);
      if (CKV_ORDER == 0) bar0();
      dma_run<8, 1024, RS>(dc, lane * 16, c1, cl + 8 * RS);
    } else if (CKV_ORDER == 2) {
      dma_run<4, 1024, RS>(dc, lane * 16, c0, cl);
      bar0();
      dma_zip<4, 1024, RS, 4 * RS>(dc, lane * 16, c0 + 4096, c1, cl + 4 * RS);
      bar0();
      dma_run<4, 1024, RS>(dc, lane * 16, c1 + 4096, cl + 12 * RS);
    } else {
      dma_zip<8, 1024, RS, 8 * RS>(dc, lane * 16, c0, c1, cl);
    }
  }
  GET_TAG();
  asm volatile("" ::"s"(junk));
  ST(2);
  v4u kf[16], vf[16];
  {
    const int vo = lane * 16, so = WS_QA + (b * 32 + 16 * hg + 2 * w) * 2048, sb = so + 131072;
    DRAIN();
    bar0();
    ST(3);
    if (QG_DELAY) __builtin_amdgcn_s_sleep(QG_DELAY);
    v4u q0 = ldc(rws, vo, so), q1 = ldc(rws, vo + 1024, so), q2 = ldc(rws, vo + 2048, so), q3 = ldc(rws, vo + 3072, so);
    v4u q4 = ldc(rws, vo, sb), q5 = ldc(rws, vo + 1024, sb), q6 = ldc(rws, vo + 2048, sb), q7 = ldc(rws, vo + 3072, sb);
    SCHED();
#pragma unroll
    for (int s = 0; s < 16; ++s) kf[s] = *(LAS v4u*)(L + kr + s * 64);
#pragma unroll
    for (int s = 0; s < 4; ++s)
#pragma unroll
      for (int t = 0; t < 4; ++t)
        vf[s * 4 + t] = cat(trd(L + (s < 2 ? vr0 : vr1) + (s & 1) * 32 * RS + t * 32), trd(L + (s < 2 ? vr0 : vr1) + ((s & 1) * 32 + 16) * RS + t * 32));
    SCHED();
    settle4(q0, q1, q2, q3, tag, vo, dws, so);
    f4 u0 = unpk(q0), u1 = unpk(q1), u2 = unpk(q2), u3 = unpk(q3);
    PIN(u0); PIN(u1); PIN(u2); PIN(u3);
    SCHED();
    settle4(q4, q5, q6, q7, tag, vo, dws, sb);
    ST(4);
    u0 += unpk(q4);
    u1 += unpk(q5);
    u2 += unpk(q6);
    u3 += unpk(q7);
    *(LAS v2u*)(L + qfw) = v2u{pk(u0[0], u0[1]), pk(u0[2], u0[3])};
    *(LAS v2u*)(L + qfw + 512) = v2u{pk(u1[0], u1[1]), pk(u1[2], u1[3])};
    *(LAS v2u*)(L + qfw + RS) = v2u{pk(u2[0], u2[1]), pk(u2[2], u2[3])};
    *(LAS v2u*)(L + qfw + RS + 512) = v2u{pk(u3[0], u3[1]), pk(u3[2], u3[3])};
  }
  ST(5);
  bar();
  f4 sa = {0.f, 0.f, 0.f, 0.f};
  v4u wv[5];
  const v3u xd = __builtin_amdgcn_raw_buffer_load_b96(mk(x, 2560), ((n >> 3) * 1280 + frow * 2) & ~3, 0, 0);
  {
    v4u qf[16];
#pragma unroll
    for (int s = 0; s < 16; ++s) qf[s] = *(LAS v4u*)(L + qfr + s * 64);
    SCHED();
#pragma unroll
    for (int s = 0; s < 16; ++s) {
      sa = mfma(kf[s], qf[s], sa);
      if (s % 3 == 0 && s < 15) {
        wv[s / 3] = *(const GAS v4u*)((s < 9 ? wp : wp2) + ((s / 3) % 3 - 1) * 3456);
        SCHED();
      }
    }
  }
  {
    float m = fmaxf(fmaxf(sa[0], sa[1]), fmaxf(sa[2], sa[3]));
    auto s1 = __builtin_amdgcn_permlane16_swap(__float_as_uint(m), __float_as_uint(m), false, false);
    m = max2(__uint_as_float(s1[0]), __uint_as_float(s1[1]));
    *(LAS float*)(L + mxw) = m;
  }
  ST(6);
  bar();
  float M;
  {
    const f4 m0 = *(LAS f4*)(L + mxr), m1 = *(LAS f4*)(L + mxr + 16), m2 = *(LAS f4*)(L + mxr + 32), m3 = *(LAS f4*)(L + mxr + 48);
    M = max3(max3(max3(m0[0], m0[1], m0[2]), max3(m0[3], m1[0], m1[1]), max3(m1[2], m1[3], m2[0])), max3(m2[1], m2[2], m2[3]),
             max2(max3(m3[0], m3[1], m3[2]), m3[3]));
    const u32 p01 = pk(__builtin_amdgcn_exp2f(sa[0] - M), __builtin_amdgcn_exp2f(sa[1] - M));
    const u32 p23 = pk(__builtin_amdgcn_exp2f(sa[2] - M), __builtin_amdgcn_exp2f(sa[3] - M));
    *(LAS v2u*)(L + pfw) = v2u{p01, p23};
  }
  bar();
  ST(7);
  {
    v4u pf[4];
#pragma unroll
    for (int s = 0; s < 4; ++s) pf[s] = *(LAS v4u*)(L + pfr + s * 1024);
    SCHED();
    if (w == 0) {
      const v4u one = {0x3F803F80u, 0x3F803F80u, 0x3F803F80u, 0x3F803F80u};
      f4 l = {0.f, 0.f, 0.f, 0.f};
#pragma unroll
      for (int s = 0; s < 4; ++s) l = mfma(one, pf[s], l);
      stc(v4u{__float_as_uint(M), tag, __float_as_uint(l[0]), tag}, rws, mlo);
    }
    SCHED();
    f4 o[4];
#pragma unroll
    for (int t = 0; t < 4; ++t) o[t] = f4{0.f, 0.f, 0.f, 0.f};
#pragma unroll
    for (int s = 0; s < 4; ++s)
#pragma unroll
      for (int t = 0; t < 4; ++t) o[t] = mfma(pf[s], vf[s * 4 + t], o[t]);
    u32 pa[4], pb[4];
#pragma unroll
    for (int k = 0; k < 4; ++k) {
      pa[k] = pk(o[0][k], o[1][k]);
      pb[k] = pk(o[2][k], o[3][k]);
    }
#pragma unroll
    for (int k = 0; k < 2; ++k) {
      v4u pc;
      pc.x = __builtin_amdgcn_update_dpp(pa[k], pa[2 + k], ROR8, 0xf, 0xc, false);
      pc.y = __builtin_amdgcn_update_dpp(pb[k], pb[2 + k], ROR8, 0xf, 0xc, false);
      pc.z = __builtin_amdgcn_update_dpp(pa[2 + k], pa[k], ROR8, 0xf, 0x3, false);
      pc.w = __builtin_amdgcn_update_dpp(pb[2 + k], pb[k], ROR8, 0xf, 0x3, false);
      stc(pc, rws, k ? o_v1 : o_vo);
    }
  }
  ST(8);
  if (u < 108) {
    bar0();
    v4u dv[4];
    {
      const rsrc_t rd = mk(wd, 1728 * 1280);
      const int so = w < 5 ? u * 20480 + w * 256 : OOB;
#pragma unroll
      for (int t = 0; t < 4; ++t) dv[t] = __builtin_amdgcn_raw_buffer_load_b128(rd, g * 4 * 1280 + n * 16 + t * 1280, so, 0);
      SCHED();
    }
    const v4u sm = selmask(n);
    const u32 sh = (g & 1) << 4;
    const u32 a0 = __builtin_amdgcn_alignbit(xd.y, xd.x, sh), a1 = __builtin_amdgcn_alignbit(xd.z, xd.y, sh), a2 = xd.z >> sh;
    f4 acc = mfma(sel(duplo(a0), sm), wv[0], f4{0.f, 0.f, 0.f, 0.f});
    acc = mfma(sel(duphi(a0), sm), wv[1], acc);
    acc = mfma(sel(duplo(a1), sm), wv[2], acc);
    acc = mfma(sel(duphi(a1), sm), wv[3], acc);
    acc = mfma(sel(duplo(a2), sm), wv[4], acc);
    *(LAS f4*)(L + L2_QF + w * 1024 + lane * 16) = acc;
    bar();
    if (w == 6) *(LAS float*)(L + L2_MX + (lane >> 5) * 4) = 1.f - mix_gate(x, gw, lane);
    if (w == 7) {
      f4 e[8];
#pragma unroll
      for (int k = 0; k < 8; ++k) e[k] = *(LAS f4*)(L + L2_QF + k * 1024 + lane * 16);
      SCHED();
      f4 s = ((e[0] + e[1]) + (e[2] + e[3])) + ((e[4] + e[5]) + (e[6] + e[7]));
      ST(9);
      s = exchange4(s, rws, dws, u, lane, tag);
      float hv[4];
#pragma unroll
      for (int k = 0; k < 4; ++k) hv[k] = s[k] * dpp<ROR8>(s[k]) * __builtin_amdgcn_rcpf(1.f + __expf(-s[k]));
      *(LAS v2u*)(L + L2_H + (g >> 1) * 256 + n * 16 + (g & 1) * 8) = v2u{pk(hv[0], hv[1]), pk(hv[2], hv[3])};
      ST(10);
    }
    int hr = L2_H + (n >> 3) * 256 + (j & 3) * 32 + g * 8, fo = WS_FF + u * 5120 + w * 512 + (g >> 1) * 2560 + n * 32 + (g & 1) * 16;
    PIN(hr); PIN(fo);
    bar();
    if (w < 5) {
      const v2u hh = *(LAS v2u*)(L + hr);
      const float og = *(LAS float*)(L + L2_MX + (lane >> 5) * 4);
      f4 d = mfma(sel(duplo(hh.x), sm), dv[0], f4{0.f, 0.f, 0.f, 0.f});
      d = mfma(sel(duphi(hh.x), sm), dv[1], d);
      d = mfma(sel(duplo(hh.y), sm), dv[2], d);
      d = mfma(sel(duphi(hh.y), sm), dv[3], d);
      d *= og;
      stc(v4u{pk(d[0], d[1]), tag, pk(d[2], d[3]), tag}, rws, fo);
    }
    ST(11);
  }
  if (u >= 88) {
    const int T = u - 88;
    int v0, v3, st, so;
    if (w < 4) {
      so = WS_FA + T * 16384 + w * 4096;
      v0 = lane * 16;
      st = 1024;
      v3 = v0 + 3072;
    } else {
      so = WS_FF + T * 64 + (w - 4) * 27 * 5120;
      v0 = (lane >> 3) * 5120 + ((lane >> 2) & 1) * 2560 + (lane & 3) * 16;
      st = 8 * 5120;
      v3 = lane < 24 ? v0 + 24 * 5120 : v0;
    }
    int v1 = v0 + st, v2 = v1 + st;
    int fw = L2_FR + w * 128 + (lane & 7) * 16;
    int fr = L2_FR + n * 8 + (g & 1) * 4, fr2 = L2_FR + 512 + (g & 1) * 64 + n * 4;
    PIN(fr); PIN(fr2);
    fr = (lane & 32) ? fr2 : fr;
    int ovo = ((lane & 32) << 25) | ((g & 1) * 1280 + n * 2 + T * 32);
    u32 keep = (w >= 4 && lane >= 24) ? 0u : ~0u;
    const u32 o1 = 0x3F80u << ((n & 1) << 4);
    v4u pick = {(n & 2) ? 0u : o1, 0u, (n & 2) ? o1 : 0u, 0u};
    PIN(v0); PIN(v1); PIN(v2); PIN(v3); PIN(fw); PIN(fr); PIN(ovo); PIN(pick); PIN(keep);
    const rsrc_t ro = mk(out, 2560);
    if (FA_REP) {
      const int rso = HEADSUP && w < 4 ? WS_HU + (32 * w + (T & 31)) * 128 : so;
      v4u a = ldc(rws, 0, rso);
#pragma unroll 1
      for (u32 it = 0; it < SPIN_CAP; ++it) {
        if (!tagbad(a, tag)) break;
        reload1(a, ~0ull, 0, dws, rso);
      }
    }
    ST(12);
    asm volatile("" ::: "memory");
    v4u a0 = ldc(rws, v0, so), a1 = ldc(rws, v1, so), a2 = ldc(rws, v2, so), a3 = ldc(rws, v3, so);
    if (__builtin_expect(!alltag(tag, a0, a1, a2, a3), 0)) {
#pragma unroll 1
      for (u32 it = 0; it < SPIN_CAP; ++it) {
        const u64 m0 = tagbad(a0, tag), m1 = tagbad(a1, tag), m2 = tagbad(a2, tag), m3 = tagbad(a3, tag);
        if (!(m0 | m1 | m2 | m3)) break;
        reload4v(a0, a1, a2, a3, m0, m1, m2, m3, v0, v1, v2, v3, dws, so);
      }
    }
    ST(13);
    a3.x &= keep;
    a3.z &= keep;
    f4 v = mfma(pick, a0, f4{0.f, 0.f, 0.f, 0.f});
    v = mfma(pick, a1, v);
    v = mfma(pick, a2, v);
    v = mfma(pick, a3, v);
#pragma unroll
    for (int k = 0; k < 4; ++k) v[k] += dpp<ROR8>(v[k]);
    *(LAS f4*)(L + fw) = v;
    bar();
    if (w == 0) {
      float A = (*(LAS float*)(L + fr) + *(LAS float*)(L + fr + 128)) + (*(LAS float*)(L + fr + 256) + *(LAS float*)(L + fr + 384));
      auto t = __builtin_amdgcn_permlane32_swap(__float_as_uint(A), __float_as_uint(A), false, false);
      A = __uint_as_float(t[0]) + __uint_as_float(t[1]);
      __builtin_amdgcn_raw_buffer_store_b16((u16)pk(A, A), ro, ovo, 0, 0);
      if (T == 0) stc1(epoch, rws, lane == 0 ? WS_EPOCH : OOB, 0);
    }
    ST(14);
  }
  STEND();
}

extern "C" void kernel_launch(void* const* d_in, const int* in_sizes, int n_in, void* d_out, int out_size, void* d_ws, size_t ws_size,
                              hipStream_t stream) {
  mla_ffn_mix<<<256, 512, 0, stream>>>((const u16*)d_in[0], (const u16*)d_in[1], (const u16*)d_in[2], (const u16*)d_in[3],
                                       (const u16*)d_in[4], (const u16*)d_in[5], (const u16*)d_in[7], (const u16*)d_in[8],
                                       (const u16*)d_in[9], (const u16*)d_in[10], (u16*)d_out, (unsigned char*)d_ws);
}
```

```cpp
#define LP_BUF wo
#define LP_OFF 0
#define LP_STEP 77952
#include <hip/hip_runtime.h>
#include <stdint.h>

typedef unsigned u32;
typedef unsigned long long u64;
typedef unsigned short u16;
typedef u32 v4u __attribute__((ext_vector_type(4)));
typedef u32 v3u __attribute__((ext_vector_type(3)));
typedef u32 v2u __attribute__((ext_vector_type(2)));
typedef float f4 __attribute__((ext_vector_type(4)));
typedef __bf16 bf8 __attribute__((ext_vector_type(8)));
typedef __bf16 bf2 __attribute__((ext_vector_type(2)));
typedef float f2 __attribute__((ext_vector_type(2)));
typedef short s4 __attribute__((ext_vector_type(4)));
typedef short s2 __attribute__((ext_vector_type(2)));
#define GAS __attribute__((address_space(1)))
#define LAS __attribute__((address_space(3)))
typedef __amdgpu_buffer_rsrc_t rsrc_t;

constexpr int AUX_SC1 = 16;
#define CODE_BYTES 13056
#define G2_DELAY 32
#define G1_DELAY 20
#define QG_DELAY 0
#define FA_REP 1
#define BADCOL 1
#define PROBE_THR 130
#define UK_LAST 1
#define HMAP 1
#define CLEAN_DELAY 0
#define CKV_ORDER 0
#define QA_TOUCH 1
#define FFN_TOUCH 1
#define HEADSUP 0
#define PACE 63
constexpr u32 SPIN_CAP = 1u << 16;
constexpr u32 SENT = 0xFFFFFFFFu;
constexpr int OOB = 0x40000000;

constexpr int WS_EPOCH = 0;
constexpr int WS_ML = 1024;
constexpr int WS_HU = 40960;
constexpr int WS_QP = 65536;
constexpr int WS_QA = WS_QP + 128 * 1024;
constexpr int WS_FF = WS_QA + 256 * 1024;
constexpr int WS_FA = WS_FF + 108 * 5120;
constexpr int WS_OB = 2 * 1024 * 1024;
constexpr int WS_END = WS_OB + 2 * 1024 * 1024;
static_assert(WS_FA + 40 * 128 * 128 <= WS_OB, "");

constexpr int RS = 1056;
constexpr int L2_QF = 128 * RS;
constexpr int L2_PF = L2_QF + 16 * RS;
constexpr int L2_MX = L2_PF + 4096;
constexpr int L2_FR = L2_MX + 1280;
constexpr int L2_H = L2_FR + 1024;
constexpr int L_DUMP = L2_H + 512;
constexpr int LDS_BYTES = L_DUMP + 512;
constexpr int L1_UV = 0, L1_WO = 32768, L1_X = L1_WO + 81920, L1_LA = L1_X + 8192, L1_CX = L1_LA + 4 * 528, L1_Q = L1_CX + 4 * 144,
              L1_W = L1_Q + 16 * 144, L1_H = L1_W + 128;
static_assert(L1_H + 512 <= L_DUMP && LDS_BYTES <= 163840, "");

__device__ __forceinline__ f4 mfma(v4u a, v4u b, f4 c) {
  return __builtin_amdgcn_mfma_f32_16x16x32_bf16(__builtin_bit_cast(bf8, a), __builtin_bit_cast(bf8, b), c, 0, 0, 0);
}
__device__ __forceinline__ f4 mfma16(v2u a, v2u b, f4 c) {
  return __builtin_amdgcn_mfma_f32_16x16x16bf16_1k(__builtin_bit_cast(s4, a), __builtin_bit_cast(s4, b), c, 0, 0, 0);
}
__device__ __forceinline__ u32 pk(float lo, float hi) {
  f2 v = {lo, hi};
  return __builtin_bit_cast(u32, __builtin_convertvector(v, bf2));
}
__device__ __forceinline__ float blo(u32 v) { return __uint_as_float(v << 16); }
__device__ __forceinline__ float bhi(u32 v) { return __uint_as_float(v & 0xFFFF0000u); }
template <int CTRL> __device__ __forceinline__ float dpp(float x) {
  return __builtin_bit_cast(float, __builtin_amdgcn_mov_dpp(__builtin_bit_cast(int, x), CTRL, 0xf, 0xf, true));
}
constexpr int XOR1 = 0xB1, XOR2 = 0x4E, ROR8 = 0x128, ROR4 = 0x124;
__device__ __forceinline__ float max3(float a, float b, float c) {
  float r;
  asm("v_max3_f32 %0, %1, %2, %3" : "=v"(r) : "v"(a), "v"(b), "v"(c));
  return r;
}
__device__ __forceinline__ float max2(float a, float b) {
  float r;
  asm("v_max_f32 %0, %1, %2" : "=v"(r) : "v"(a), "v"(b));
  return r;
}
__device__ __forceinline__ float x16_max(float x) {
  auto s = __builtin_amdgcn_permlane16_swap(__float_as_uint(x), __float_as_uint(x), false, false);
  return fmaxf(__uint_as_float(s[0]), __uint_as_float(s[1]));
}
__device__ __forceinline__ float x16_sum(float x) {
  auto s = __builtin_amdgcn_permlane16_swap(__float_as_uint(x), __float_as_uint(x), false, false);
  return __uint_as_float(s[0]) + __uint_as_float(s[1]);
}
__device__ __forceinline__ float x32_max(float x) {
  auto t = __builtin_amdgcn_permlane32_swap(__float_as_uint(x), __float_as_uint(x), false, false);
  return fmaxf(__uint_as_float(t[0]), __uint_as_float(t[1]));
}
__device__ __forceinline__ rsrc_t mk(const void* p, u32 bytes) { return __builtin_amdgcn_make_buffer_rsrc((void*)p, 0, bytes, 0x00020000); }
__device__ __forceinline__ v4u mkd(const void* p, u32 bytes) {
  return v4u{(u32)(size_t)p, (u32)((size_t)p >> 32) & 0xFFFFu, bytes, 0x00020000u};
}
__device__ __forceinline__ v4u ldc(rsrc_t r, int vo, int so) { return __builtin_amdgcn_raw_buffer_load_b128(r, vo, so, AUX_SC1); }
__device__ __forceinline__ void stc(v4u v, rsrc_t r, int vo) { __builtin_amdgcn_raw_buffer_store_b128(v, r, vo, 0, AUX_SC1); }
__device__ __forceinline__ void stc2(v2u v, rsrc_t r, int vo, int so) { __builtin_amdgcn_raw_buffer_store_b64(v, r, vo, so, AUX_SC1); }
__device__ __forceinline__ void stc1(u32 v, rsrc_t r, int vo, int so) { __builtin_amdgcn_raw_buffer_store_b32(v, r, vo, so, AUX_SC1); }
template <int N, int SSTEP, int LSTEP, int KEEP = 63> __device__ __forceinline__ void dma_run(v4u d, int vo, int so, int lds) {
  int t;
  asm volatile("s_mov_b32 m0, %[l]\n\ts_mov_b32 %[t], %[s]\n\t.rept %[n]\n\tbuffer_load_dwordx4 %[v], %[d], %[t] offen lds\n\t"
               "s_add_u32 m0, m0, %[ls]\n\ts_add_u32 %[t], %[t], %[ss]\n\t.if %[k] < 63\n\ts_waitcnt vmcnt(%[k])\n\t.endif\n\t.endr"
               : [t] "=&s"(t)
               : [v] "v"(vo), [d] "s"(d), [s] "s"(so), [l] "s"(lds), [n] "i"(N), [ls] "i"(LSTEP), [ss] "i"(SSTEP), [k] "i"(KEEP)
               : "memory", "scc");
}
template <int N, int SSTEP, int LSTEP> __device__ __forceinline__ void dma_run2(v4u d, int va, int vb, int so, int lds) {
  int t;
  asm volatile("s_mov_b32 m0, %[l]\n\ts_mov_b32 %[t], %[s]\n\t.rept %[n]\n\tbuffer_load_dwordx4 %[va], %[d], %[t] offen lds\n\t"
               "s_add_u32 m0, m0, 0x400\n\ts_nop 0\n\tbuffer_load_dwordx4 %[vb], %[d], %[t] offen lds\n\t"
               "s_add_u32 m0, m0, %[ls]\n\ts_add_u32 %[t], %[t], %[ss]\n\t.endr"
               : [t] "=&s"(t)
               : [va] "v"(va), [vb] "v"(vb), [d] "s"(d), [s] "s"(so), [l] "s"(lds), [n] "i"(N), [ls] "i"(LSTEP - 1024), [ss] "i"(SSTEP)
               : "memory", "scc");
}
template <int N, int SSTEP, int LSTEP, int LD> __device__ __forceinline__ void dma_zip(v4u d, int vo, int sa, int sb, int lds) {
  int t, t2;
  asm volatile("s_mov_b32 m0, %[l]\n\ts_mov_b32 %[t], %[sa]\n\ts_mov_b32 %[u], %[sb]\n\t.rept %[n]\n\t"
               "buffer_load_dwordx4 %[v], %[d], %[t] offen lds\n\ts_add_u32 m0, m0, %[ld]\n\ts_add_u32 %[t], %[t], %[ss]\n\t"
               "buffer_load_dwordx4 %[v], %[d], %[u] offen lds\n\ts_sub_u32 m0, m0, %[lb]\n\ts_add_u32 %[u], %[u], %[ss]\n\t.endr"
               : [t] "=&s"(t), [u] "=&s"(t2)
               : [v] "v"(vo), [d] "s"(d), [sa] "s"(sa), [sb] "s"(sb), [l] "s"(lds), [n] "i"(N), [ld] "i"(LD), [lb] "i"(LD - LSTEP), [ss] "i"(SSTEP)
               : "memory", "scc");
}
__device__ __forceinline__ void touch(v4u d, int vo, int so, int lds) {
  asm volatile("s_mov_b32 m0, %[l]\n\ts_nop 0\n\tbuffer_load_dword %[v], %[d], %[s] offen lds" ::[v] "v"(vo), [d] "s"(d), [s] "s"(so), [l] "s"(lds)
               : "memory");
}
__device__ __forceinline__ v2u trd(LAS unsigned char* p) {
  return __builtin_bit_cast(v2u, __builtin_amdgcn_ds_read_tr16_b64_v4i16((LAS s4*)p));
}
__device__ __forceinline__ v4u cat(v2u a, v2u b) { return v4u{a.x, a.y, b.x, b.y}; }
#define SCHED() __builtin_amdgcn_sched_barrier(0)
#define PIN(x) asm volatile("" : "+v"(x))
#define PINS(x) asm volatile("" : "+s"(x))
__device__ __forceinline__ void bar() {
  asm volatile("s_waitcnt lgkmcnt(0)" ::: "memory");
  __builtin_amdgcn_s_barrier();
  asm volatile("" ::: "memory");
}
__device__ __forceinline__ void bar0() {
  SCHED();
  asm volatile("" ::: "memory");
  __builtin_amdgcn_s_barrier();
  asm volatile("" ::: "memory");
  SCHED();
}
#define VMW(n) asm volatile("s_waitcnt vmcnt(" #n ")" ::: "memory")
#define DRAIN() VMW(0)
__device__ __forceinline__ u64 tagbad(v4u a, u32 tag) { return __builtin_amdgcn_ballot_w64(a.y != tag) | __builtin_amdgcn_ballot_w64(a.w != tag); }
__device__ __forceinline__ u64 sentbad(v4u a) { return __builtin_amdgcn_ballot_w64(max(max(a.x, a.y), max(a.z, a.w)) == SENT); }
#define CX(i) "v_cmpx_eq_u32 vcc, %[t], %[a" #i "]\n\t"
__device__ __forceinline__ bool alltag(u32 tag, v4u p, v4u q, v4u r, v4u s) {
  u64 e;
  asm volatile(CX(0) CX(1) CX(2) CX(3) CX(4) CX(5) CX(6) CX(7) "s_mov_b64 %[e], exec\n\ts_mov_b64 exec, -1\n\ts_nop 1"
               : [e] "=s"(e)
               : [t] "s"(tag), [a0] "v"(p.y), [a1] "v"(p.w), [a2] "v"(q.y), [a3] "v"(q.w), [a4] "v"(r.y), [a5] "v"(r.w), [a6] "v"(s.y), [a7] "v"(s.w)
               : "vcc");
  return e == ~0ull;
}
#define RL1(i) "s_mov_b64 exec, %[m" #i "]\n\ts_cbranch_execz " #i "f\n\tbuffer_load_dwordx4 %[a" #i "], %[v" #i "], %[rs], %[so] offen offset:%[o" #i "] sc1\n" #i ":\n\t"
__device__ __forceinline__ void reload4(v4u& a1, v4u& a2, v4u& a3, v4u& a4, u64 m1, u64 m2, u64 m3, u64 m4, int v, v4u rs, int so) {
  asm volatile("s_nop 4\n\t" RL1(1) RL1(2) RL1(3) RL1(4) "s_mov_b64 exec, -1\n\ts_waitcnt vmcnt(0)"
               : [a1] "+v"(a1), [a2] "+v"(a2), [a3] "+v"(a3), [a4] "+v"(a4)
               : [m1] "s"(m1), [m2] "s"(m2), [m3] "s"(m3), [m4] "s"(m4), [v1] "v"(v), [v2] "v"(v), [v3] "v"(v), [v4] "v"(v), [rs] "s"(rs),
                 [so] "s"(so), [o1] "i"(0), [o2] "i"(1024), [o3] "i"(2048), [o4] "i"(3072)
               : "memory");
}
__device__ __forceinline__ void reload3(v4u& a1, v4u& a2, v4u& a3, u64 m1, u64 m2, u64 m3, int v1, int v2, int v3, v4u rs, int so) {
  asm volatile("s_nop 4\n\t" RL1(1) RL1(2) RL1(3) "s_mov_b64 exec, -1\n\ts_waitcnt vmcnt(0)"
               : [a1] "+v"(a1), [a2] "+v"(a2), [a3] "+v"(a3)
               : [m1] "s"(m1), [m2] "s"(m2), [m3] "s"(m3), [v1] "v"(v1), [v2] "v"(v2), [v3] "v"(v3), [rs] "s"(rs), [so] "s"(so), [o1] "i"(0),
                 [o2] "i"(0), [o3] "i"(0)
               : "memory");
}
__device__ __forceinline__ void reload1(v4u& a1, u64 m1, int v1, v4u rs, int so) {
  asm volatile("s_nop 4\n\t" RL1(1) "s_mov_b64 exec, -1\n\ts_waitcnt vmcnt(0)"
               : [a1] "+v"(a1)
               : [m1] "s"(m1), [v1] "v"(v1), [rs] "s"(rs), [so] "s"(so), [o1] "i"(0)
               : "memory");
}
__device__ __forceinline__ void reload4v(v4u& a1, v4u& a2, v4u& a3, v4u& a4, u64 m1, u64 m2, u64 m3, u64 m4, int v1, int v2, int v3, int v4, v4u rs,
                                         int so) {
  asm volatile("s_nop 4\n\t" RL1(1) RL1(2) RL1(3) RL1(4) "s_mov_b64 exec, -1\n\ts_waitcnt vmcnt(0)"
               : [a1] "+v"(a1), [a2] "+v"(a2), [a3] "+v"(a3), [a4] "+v"(a4)
               : [m1] "s"(m1), [m2] "s"(m2), [m3] "s"(m3), [m4] "s"(m4), [v1] "v"(v1), [v2] "v"(v2), [v3] "v"(v3), [v4] "v"(v4), [rs] "s"(rs),
                 [so] "s"(so), [o1] "i"(0), [o2] "i"(0), [o3] "i"(0), [o4] "i"(0)
               : "memory");
}
__device__ __forceinline__ void settle4(v4u& a, v4u& b, v4u& c, v4u& d, u32 tag, int v, v4u rs, int so) {
  if (__builtin_expect(!alltag(tag, a, b, c, d), 0)) {
#pragma unroll 1
    for (u32 it = 0; it < SPIN_CAP; ++it) {
      const u64 m0 = tagbad(a, tag), m1 = tagbad(b, tag), m2 = tagbad(c, tag), m3 = tagbad(d, tag);
      if (!(m0 | m1 | m2 | m3)) break;
      __builtin_amdgcn_s_sleep(2);
      reload4(a, b, c, d, m0, m1, m2, m3, v, rs, so);
    }
  }
}
__device__ __forceinline__ v4u sel(u32 dup, v4u m) { return v4u{dup & m.x, dup & m.y, dup & m.z, dup & m.w}; }
__device__ __forceinline__ v4u selmask(int n) {
  const u32 v = 0xFFFFu << ((n & 1) << 4);
  const int q = (n >> 1) & 3;
  return v4u{q == 0 ? v : 0u, q == 1 ? v : 0u, q == 2 ? v : 0u, q == 3 ? v : 0u};
}
__device__ __forceinline__ u32 duplo(u32 d) { return __builtin_amdgcn_perm(d, d, 0x01000100u); }
__device__ __forceinline__ u32 duphi(u32 d) { return __builtin_amdgcn_perm(d, d, 0x03020302u); }
__device__ __forceinline__ f4 unpk(v4u a) { return f4{blo(a.x), bhi(a.x), blo(a.z), bhi(a.z)}; }

__device__ __forceinline__ f4 exchange4(f4 s, rsrc_t rws, v4u dws, int slot, int lane, u32 tag) {
  stc(v4u{pk(s[0], s[1]), tag, pk(s[2], s[3]), tag}, rws, WS_QP + slot * 1024 + lane * 16);
  const int v0 = (slot & 3) * 1024 + lane * 16, so = WS_QP + (slot & ~3) * 1024;
  DRAIN();
  v4u q1 = ldc(rws, v0 ^ 1024, so), q2 = ldc(rws, v0 ^ 2048, so), q3 = ldc(rws, v0 ^ 3072, so);
#pragma unroll 1
  for (u32 it = 0; it < SPIN_CAP; ++it) {
    const u64 m1 = tagbad(q1, tag), m2 = tagbad(q2, tag), m3 = tagbad(q3, tag);
    if (!(m1 | m2 | m3)) break;
    __builtin_amdgcn_s_sleep(2);
    reload3(q1, q2, q3, m1, m2, m3, v0 ^ 1024, v0 ^ 2048, v0 ^ 3072, dws, so);
  }
  return (s + unpk(q1)) + (unpk(q2) + unpk(q3));
}

__device__ __forceinline__ float mix_gate(const u16* x, const u16* gw, int lane) {
  const rsrc_t rx = mk(x, 2560), rg = mk(gw, 1280);
  v2u a[5], c[5];
#pragma unroll
  for (int k = 0; k < 5; ++k) {
    a[k] = __builtin_amdgcn_raw_buffer_load_b64(rx, (lane >> 5) * 1280 + (lane & 31) * 8 + k * 256, 0, 0);
    c[k] = __builtin_amdgcn_raw_buffer_load_b64(rg, (lane & 31) * 8 + k * 256, 0, 0);
  }
  float s = 0.f;
#pragma unroll
  for (int k = 0; k < 5; ++k) s += (blo(a[k].x) * blo(c[k].x) + bhi(a[k].x) * bhi(c[k].x)) + (blo(a[k].y) * blo(c[k].y) + bhi(a[k].y) * bhi(c[k].y));
  s += dpp<XOR1>(s);
  s += dpp<XOR2>(s);
  s += dpp<ROR4>(s);
  s += dpp<ROR8>(s);
  return 1.f / (1.f + __expf(-x16_sum(s)));
}

#define ST(i)
#define STEND()

extern "C" __global__ void __launch_bounds__(512, 2)
mla_ffn_mix(const u16* __restrict__ x, const u16* __restrict__ ckv, const u16* __restrict__ wq, const u16* __restrict__ wuk,
            const u16* __restrict__ wuv, const u16* __restrict__ wo, const u16* __restrict__ wg, const u16* __restrict__ wu,
            const u16* __restrict__ wd, const u16* __restrict__ gw, u16* __restrict__ out, unsigned char* __restrict__ ws) {
  __shared__ __attribute__((aligned(16))) unsigned char smem[LDS_BYTES];
  LAS unsigned char* L = (LAS unsigned char*)smem;
  const int LB = (int)(u32)(size_t)L;
  ST(0);
  const int tid = threadIdx.x, bid = blockIdx.x;
  u64 kbase;
  asm volatile("s_getpc_b64 %0\n\ts_andn2_b64 %0, %0, 0xff" : "=s"(kbase)::"scc");
  const int lane = tid & 63, w = __builtin_amdgcn_readfirstlane(tid >> 6);
  const int xcd = bid & 7, r = bid >> 3;
  const int n = lane & 15, g = lane >> 4;
  const int h = (HMAP ? (xcd & 3) * 8 + (xcd & 4) : 4 * xcd) + (r >> 2), i = r & 3, dh = i >> 1, ch = i & 1;
  const int lr = lane >> 3, ckx = ((lane & 7) ^ (lr & 6)) << 4;
  const int col = h * 256 + dh * 128, r0 = 80 * (w ^ (4 * ch));
  int qva = ((lr >> 2) * 20 + (lr & 3)) * 8192 + ckx, qvb = qva + 40 * 8192, qso = r0 * 8192 + col, qld = LB + L1_WO + w * 10240;
  PIN(qva); PIN(qvb); PINS(qso); PINS(qld);
  SCHED();
  asm volatile("" ::"s"(x), "s"(ckv), "s"(wq), "s"(wuk), "s"(wuv), "s"(wo), "s"(wg), "s"(wu), "s"(wd), "s"(gw), "s"(out), "s"(ws));
  u32 lat_ = 0;
  {
    const u64 b0 = __builtin_amdgcn_s_memrealtime();
    if (bid == 197 && w == 0) {
      const unsigned char* pb = (const unsigned char*)(LP_BUF) + LP_OFF;
      u32 acc = 0;
      for (int k = 0; k < 64; ++k) {
        u32 v;
        const unsigned char* a = pb + (size_t)k * LP_STEP + (acc & 0);
        asm volatile("global_load_dword %0, %1, off\n s_waitcnt vmcnt(0)" : "=v"(v) : "v"(a) : "memory");
        acc += v;
      }
      asm volatile("" :: "v"(acc));
      lat_ = (u32)(__builtin_amdgcn_s_memrealtime() - b0);
    }
    while (__builtin_amdgcn_s_memrealtime() < b0 + 5000) __builtin_amdgcn_s_sleep(2);
  }
  const v4u dq = mkd(wq, 640 * 8192);
  if (bid < 128 && w < 4) dma_run2<5, 4 * 8192, 2048>(dq, qva, qvb, qso, qld);
  SCHED();
  u32 epoch, tag;
  asm volatile("s_load_dword %0, %1, 0x0" : "=s"(epoch) : "s"(ws) : "memory");
#define GET_TAG()                                                                          \
  do {                                                                                     \
    asm volatile("s_waitcnt lgkmcnt(0)" : "+s"(epoch)::"memory");                          \
    epoch += 1u;                                                                           \
    tag = (epoch & 0x3FFFu) | ((epoch << 1) & 0x3FFF8000u) | ((epoch << 2) & 0x80000000u); \
    PINS(tag);                                                                             \
  } while (0)
  const rsrc_t rws = mk(ws, WS_END);
  const v4u dws = mkd(ws, WS_END);

  if (__builtin_expect(r < 16, 1)) {
    const int q = n >> 2, p = n & 3, sw = 2 * (g & 1) + (q >> 1), rb = (4 * g + q) * 128 + p * 8;
    {
      const v4u dk = mkd(wuk, 512 * 8192);
      int ta[4];
#pragma unroll
      for (int t = 0; t < 4; ++t) {
        ta[t] = L1_WO + w * 10240 + rb + ((t ^ sw) << 5);
        PIN(ta[t]);
      }
      int kvo = lr * 8192 + ckx, xvo = (lane >> 5) * 1280 + (lane & 31) * 16;
      PIN(kvo); PIN(xvo);
      bar0();
      if (w >= 4) dma_run2<5, 4 * 8192, 2048>(dq, qva, qvb, qso, qld);
      dma_run<1, 0, 0>(mkd(x, 2560), xvo, r0 * 2, LB + L1_X + w * 1024);
      GET_TAG();
      int qw = L1_Q + (8 * (g >> 1) + w) * 144 + (g & 1) * 64 + n * 2, qr = L1_Q + n * 144 + g * 16;
      int svo = ((lane & 16) << 26) | ((g >> 1) * 65536 + n * 8), sso = WS_QA + dh * 131072 + h * 2048 + ch * 1024 + w * 128;
      int xr = L1_X + w * 1024 + (n >> 3) * 512 + g * 40, ur0 = L1_UV + w * 4096 + n * 256 + ((g ^ (p << 1)) << 4), ur1 = ur0 ^ 64;
      PIN(qw); PIN(qr); PIN(svo); PINS(sso); PIN(xr); PIN(ur0); PIN(ur1);
      if (UK_LAST) bar0();
      dma_run<4, 8 * 8192, 1024>(dk, kvo, (256 * ch + 32 * w) * 8192 + col, LB + L1_UV + w * 4096);
      ST(1);
      VMW(4);
      v2u xf[5], f[20];
#pragma unroll
      for (int a = 0; a < 5; ++a) {
        xf[a] = *(LAS v2u*)(L + xr + a * 8);
#pragma unroll
        for (int t = 0; t < 4; ++t) f[a * 4 + t] = trd(L + ta[t] + a * 2048);
      }
      SCHED();
      f4 d[4];
#pragma unroll
      for (int t = 0; t < 4; ++t) d[t] = f4{0.f, 0.f, 0.f, 0.f};
#pragma unroll
      for (int a = 0; a < 5; ++a)
#pragma unroll
        for (int t = 0; t < 4; ++t) d[t] = mfma16(xf[a], f[a * 4 + t], d[t]);
      const u32 qq = pk((g & 1) ? d[2][0] : d[0][0], (g & 1) ? d[3][0] : d[1][0]);
      *(LAS u16*)(L + qw) = (u16)qq;
      *(LAS u16*)(L + qw + 32) = (u16)(qq >> 16);
      ST(2);
      SCHED();
      VMW(0);
      v4u uk[4];
#pragma unroll
      for (int k = 0; k < 4; ++k) uk[k] = *(LAS v4u*)(L + ((k & 1) ? ur1 : ur0) + (k >> 1) * 128);
      ST(12);
      bar();
      const v4u q0 = *(LAS v4u*)(L + qr), q1 = *(LAS v4u*)(L + qr + 64);
      f4 a0 = mfma(q0, uk[0], f4{0.f, 0.f, 0.f, 0.f});
      f4 a1 = mfma(q0, uk[2], f4{0.f, 0.f, 0.f, 0.f});
      a0 = mfma(q1, uk[1], a0);
      a1 = mfma(q1, uk[3], a1);
      constexpr float SC = 0.08838834764831845f * 1.4426950408889634f;
      const float v0 = x16_sum((a0[0] + a0[1]) + (a0[2] + a0[3])) * SC, v1 = x16_sum((a1[0] + a1[1]) + (a1[2] + a1[3])) * SC;
      stc2(v2u{pk(v0, v1), tag}, rws, svo, sso);
    }
    bar0();
    ST(5);
    float gate = 0.f;
    if (w == 7) {
      gate = mix_gate(x, gw, lane);
      PIN(gate);
    } else if (G1_DELAY) {
      __builtin_amdgcn_s_sleep(G1_DELAY);
    }
    bar0();
    const int b = w >> 2, cb = w & 3;
    if (QA_TOUCH && w == 0) touch(dws, lane * 128, WS_QA + (r >> 3) * 131072 + (xcd >> 2) * 65536 + (r & 7) * 8192, LB + L_DUMP);
    if (FFN_TOUCH && xcd * 16 + r < 108) {
      const int fu = xcd * 16 + r;
      if (w >= 1 && w < 7) {
        const int k = w - 1 - (w >= 4 ? 3 : 0), rr = k * 64 + lane;
        touch(mkd(w >= 4 ? wu : wg, 640 * 3456), rr < 160 ? rr * 3456 : OOB, 160 * i * 3456 + (fu >> 2) * 128, LB + L_DUMP);
      }
    }
    {
      const int uvo = lr * 8192 + (((lane & 7) ^ (w & 6)) << 4), ovo = lr * 1280 + ckx;
      dma_run<4, 64 * 8192, 8192, PACE>(mkd(wuv, 512 * 8192), uvo, (256 * ch + 8 * w) * 8192 + col, LB + L1_UV + w * 1024);
      const v4u dwo = mkd(wo, 4096 * 1280);
      const int oso = (h * 128 + 64 * dh + 8 * w) * 1280;
      dma_run<5, 128, 8192, PACE>(dwo, ovo, oso + ch * 640, LB + L1_WO + ch * 40960 + w * 1024);
      if (FFN_TOUCH && w < 3) touch(mkd(wd, 1728 * 1280), w * 64 + lane < 160 ? lane * 128 : OOB, (xcd * 16 + r) * 20480 + w * 8192, LB + L_DUMP);
      bar0();
      dma_run<5, 128, 8192>(dwo, ovo, oso + 640 - ch * 640, LB + L1_WO + 40960 - ch * 40960 + w * 1024);
    }
    ST(6);
    v4u sm = selmask(n);
    const int kg0 = 8 * g + 4 * (n >> 3);
    int vo = kg0 * 1024 + (n & 7) * 16, so = WS_OB + (b * 32 + h) * 32768 + ch * 512 + cb * 128;
    int law = L1_LA + ((g & 2) + b) * 528 + cb * 128 + (n & 7) * 16 + (g & 1) * 8, lad = L_DUMP + lane * 8;
    PIN(law); PIN(lad);
    law = (g >> 1) == (n >> 3) ? law : lad;
    int lar = L1_LA + p * 528 + b * 256 + g * 16;
    int cxw = L1_CX + 2 * b * 144 + (32 * (cb >> 1) + 8 * q + 4 * (cb & 1) + p) * 2, cxr = L1_CX + p * 144 + g * 16;
    int fvo = g * 16384 + n * 8, fv4 = lane < 16 ? n * 8 + 4 * 16384 : OOB, fso = WS_FA + 5 * w * 16384 + (h * 4 + i) * 128;
    int wr = L1_W + b * 64 + kg0 * 2;
    int va = L1_UV + b * 16384 + (16 * q + g) * 128 + p * 8 + ((cb ^ q) << 5), to[5];
#pragma unroll
    for (int t = 0; t < 5; ++t) {
      const int T = 5 * w + t;
      to[t] = L1_WO + (T >> 2) * 8192 + rb + (((T & 3) ^ sw) << 5);
      PIN(to[t]);
    }
    int huo = lane < 8 ? WS_HU + (h * 4 + i) * 128 + lane * 16 : OOB;
    PIN(huo);
    PIN(sm); PIN(vo); PINS(so); PIN(law); PIN(lar); PIN(cxw); PIN(cxr); PIN(fvo); PIN(fv4); PINS(fso); PIN(wr); PIN(va);
    SCHED();
    DRAIN();
    ST(7);
    bar();
    v4u fv[4], fo[10];
    v4u ml;
    if (w == 7) ml = ldc(rws, lane * 512, WS_ML + h * 16);
    SCHED();
    {
#pragma unroll
      for (int k = 0; k < 4; ++k) fv[k] = cat(trd(L + va + (k >> 1) * 8192 + (k & 1) * 512), trd(L + va + (k >> 1) * 8192 + (k & 1) * 512 + 1024));
#pragma unroll
      for (int t = 0; t < 5; ++t) {
        fo[2 * t] = cat(trd(L + to[t]), trd(L + to[t] + 2048));
        fo[2 * t + 1] = cat(trd(L + to[t] + 4096), trd(L + to[t] + 6144));
      }
    }
    SCHED();
    if (w == 7) {
#pragma unroll 1
      for (u32 it = 0; it < SPIN_CAP; ++it) {
        const u64 m1 = tagbad(ml, tag);
        if (!m1) break;
        reload1(ml, m1, lane * 512, dws, WS_ML + h * 16);
      }
      ST(11);
    }
    bar0();
    v4u o0 = ldc(rws, vo, so), o1 = ldc(rws, vo + 1024, so), o2 = ldc(rws, vo + 2048, so), o3 = ldc(rws, vo + 3072, so);
    SCHED();
    if (w == 7) {
      const float m = __uint_as_float(ml.x);
      float M = fmaxf(m, dpp<XOR1>(m));
      M = fmaxf(M, dpp<XOR2>(M));
      M = fmaxf(M, dpp<ROR4>(M));
      M = fmaxf(M, dpp<ROR8>(M));
      M = x16_max(M);
      const float e = __builtin_amdgcn_exp2f(m - M);
      float Ls = e * __uint_as_float(ml.z);
      Ls += dpp<XOR1>(Ls);
      Ls += dpp<XOR2>(Ls);
      Ls += dpp<ROR4>(Ls);
      Ls += dpp<ROR8>(Ls);
      const float wn = e * gate * __builtin_amdgcn_rcpf(x16_sum(Ls));
      *(LAS u16*)(L + L1_W + lane * 2) = (u16)pk(wn, wn);
    }
    bar();
    const v2u w4 = *(LAS v2u*)(L + wr);
    v4u aw0 = sel(duplo(w4.x), sm), aw1 = sel(duphi(w4.x), sm), aw2 = sel(duplo(w4.y), sm), aw3 = sel(duphi(w4.y), sm);
    PIN(aw0); PIN(aw1); PIN(aw2); PIN(aw3);
    SCHED();
    {
      const u32 ma = max(max(max(max(o0.x, o0.y), o0.z), max(max(o0.w, o1.x), o1.y)), max(o1.z, o1.w));
      const u32 mb = max(max(max(max(o2.x, o2.y), o2.z), max(max(o2.w, o3.x), o3.y)), max(o3.z, o3.w));
      if (__builtin_expect(__builtin_amdgcn_ballot_w64(max(ma, mb) == SENT) != 0ull, 0)) {
#pragma unroll 1
        for (u32 it = 0; it < SPIN_CAP; ++it) {
          const u64 m0 = sentbad(o0), m1 = sentbad(o1), m2 = sentbad(o2), m3 = sentbad(o3);
          if (!(m0 | m1 | m2 | m3)) break;
          reload4(o0, o1, o2, o3, m0, m1, m2, m3, vo, dws, so);
        }
      }
    }
    ST(8);
    if (HEADSUP == 1 && w == 0) stc(v4u{tag, tag, tag, tag}, rws, huo);
    {
      f4 acc = mfma(aw0, o0, f4{0.f, 0.f, 0.f, 0.f});
      acc = mfma(aw1, o1, acc);
      acc = mfma(aw2, o2, acc);
      acc = mfma(aw3, o3, acc);
      *(LAS v2u*)(L + law) = v2u{pk(acc[0], acc[1]), pk(acc[2], acc[3])};
    }
    bar();
    if (HEADSUP == 2 && w == 0) stc(v4u{tag, tag, tag, tag}, rws, huo);
    {
      v4u a[4];
#pragma unroll
      for (int k = 0; k < 4; ++k) a[k] = *(LAS v4u*)(L + lar + k * 64);
      f4 d = {0.f, 0.f, 0.f, 0.f};
#pragma unroll
      for (int k = 0; k < 4; ++k) d = mfma(a[k], fv[k], d);
      const u32 c = pk(d[0] + d[2], d[1] + d[3]);
      *(LAS u16*)(L + cxw) = (u16)c;
      *(LAS u16*)(L + cxw + 144) = (u16)(c >> 16);
    }
    bar();
    ST(9);
    {
      const v4u a0 = *(LAS v4u*)(L + cxr), a1 = *(LAS v4u*)(L + cxr + 64);
      u32 cv[5];
      f4 dacc[5];
#pragma unroll
      for (int t = 0; t < 5; ++t) dacc[t] = mfma(a0, fo[2 * t], f4{0.f, 0.f, 0.f, 0.f});
#pragma unroll
      for (int t = 0; t < 5; ++t) {
        dacc[t] = mfma(a1, fo[2 * t + 1], dacc[t]);
        cv[t] = pk(dacc[t][0] + dacc[t][2], dacc[t][1] + dacc[t][3]);
      }
      const u32 c01 = (g & 1) ? cv[1] : cv[0], c23 = (g & 1) ? cv[3] : cv[2];
      stc2(v2u{(g & 2) ? c23 : c01, tag}, rws, fvo, fso);
      stc2(v2u{cv[4], tag}, rws, fv4, fso);
    }
    ST(10);
    STEND();
    __builtin_amdgcn_endpgm();
  }
  const int j = r - 16, u = xcd * 16 + j, hg = r & 1, bk = u >> 1, b = bk >> 5, kg = bk & 31;
  u32 junk;
  asm volatile("s_load_dword %0, %1, %2" : "=s"(junk) : "s"(kbase), "s"(min((j * 8 + w) * 128, CODE_BYTES - 128)) : "memory");
  if (BADCOL) {
    const int cc = (HMAP ? (xcd & 3) * 8 + (xcd & 4) : 4 * xcd) * 256 + 384;
    if (w == 0) touch(dq, lane < 40 ? lane * 8192 : OOB, 40 * j * 8192 + cc, LB + L_DUMP);
    if (w == 1) touch(mkd(wuk, 512 * 8192), lane < 32 ? lane * 8192 : OOB, 32 * j * 8192 + cc, LB + L_DUMP);
    if (w == 2) touch(mkd(wuv, 512 * 8192), lane < 32 ? lane * 8192 : OOB, 32 * j * 8192 + cc, LB + L_DUMP);
  }
  int o_vo = WS_OB + (b * 32 + 16 * hg + 4 * g + 2 * (n >> 3)) * 32768 + kg * 1024 + w * 128 + (n & 7) * 16, o_v1 = o_vo + 32768;
  PIN(o_vo);
  PIN(o_v1);
  if (CLEAN_DELAY) __builtin_amdgcn_s_sleep(CLEAN_DELAY);
  stc(v4u{SENT, SENT, SENT, SENT}, rws, o_vo);
  stc(v4u{SENT, SENT, SENT, SENT}, rws, o_v1);
  int qfw = L2_QF + 2 * w * RS + lane * 8, qfr = L2_QF + n * RS + g * 16, mxw = L2_MX + n * 80 + w * 8 + (lane >> 5) * 4, mxr = L2_MX + n * 80;
  int pfw = L2_PF + (w >> 1) * 1024 + lane * 16 + (w & 1) * 8, pfr = L2_PF + lane * 16;
  int mlo = lane < 16 ? WS_ML + bk * 512 + (16 * hg + n) * 16 : OOB;
  int vr0 = (4 * g + (n >> 2)) * RS + w * 128 + (n & 3) * 8, vr1 = vr0 + 64 * RS, kr = (16 * w + n) * RS + g * 16;
  PIN(qfw); PIN(qfr); PIN(mxw); PIN(mxr); PIN(pfw); PIN(pfr); PIN(mlo); PIN(vr0); PIN(vr1); PIN(kr);
  const int frow = 160 * (j & 3) + 20 * w + 5 * g;
  const GAS unsigned char* wp = (const GAS unsigned char*)(n < 8 ? wg : wu) + (frow + 1) * 3456 + (u < 108 ? u >> 2 : 0) * 128 + (n & 7) * 16;
  const GAS unsigned char* wp2 = wp + 3 * 3456;
  asm volatile("" : "+v"(wp), "+v"(wp2));
  SCHED();
  if (G2_DELAY) __builtin_amdgcn_s_sleep(G2_DELAY);
  ST(1);
  {
    const v4u dc = mkd(ckv, 2 * 4096 * 1024);
    const int c0 = (bk * 128 + 64 * hg + 8 * w) * 1024, c1 = (bk * 128 + 64 - 64 * hg + 8 * w) * 1024, cl = LB + 16 * w * RS;
    if (CKV_ORDER < 2) {
      dma_run<8, 1024, RS>(dc, lane * 16, c0, cl);
      if (CKV_ORDER == 0) bar0();
      dma_run<8, 1024, RS>(dc, lane * 16, c1, cl + 8 * RS);
    } else if (CKV_ORDER == 2) {
      dma_run<4, 1024, RS>(dc, lane * 16, c0, cl);
      bar0();
      dma_zip<4, 1024, RS, 4 * RS>(dc, lane * 16, c0 + 4096, c1, cl + 4 * RS);
      bar0();
      dma_run<4, 1024, RS>(dc, lane * 16, c1 + 4096, cl + 12 * RS);
    } else {
      dma_zip<8, 1024, RS, 8 * RS>(dc, lane * 16, c0, c1, cl);
    }
  }
  GET_TAG();
  asm volatile("" ::"s"(junk));
  ST(2);
  v4u kf[16], vf[16];
  {
    const int vo = lane * 16, so = WS_QA + (b * 32 + 16 * hg + 2 * w) * 2048, sb = so + 131072;
    DRAIN();
    bar0();
    ST(3);
    if (QG_DELAY) __builtin_amdgcn_s_sleep(QG_DELAY);
    v4u q0 = ldc(rws, vo, so), q1 = ldc(rws, vo + 1024, so), q2 = ldc(rws, vo + 2048, so), q3 = ldc(rws, vo + 3072, so);
    v4u q4 = ldc(rws, vo, sb), q5 = ldc(rws, vo + 1024, sb), q6 = ldc(rws, vo + 2048, sb), q7 = ldc(rws, vo + 3072, sb);
    SCHED();
#pragma unroll
    for (int s = 0; s < 16; ++s) kf[s] = *(LAS v4u*)(L + kr + s * 64);
#pragma unroll
    for (int s = 0; s < 4; ++s)
#pragma unroll
      for (int t = 0; t < 4; ++t)
        vf[s * 4 + t] = cat(trd(L + (s < 2 ? vr0 : vr1) + (s & 1) * 32 * RS + t * 32), trd(L + (s < 2 ? vr0 : vr1) + ((s & 1) * 32 + 16) * RS + t * 32));
    SCHED();
    settle4(q0, q1, q2, q3, tag, vo, dws, so);
    f4 u0 = unpk(q0), u1 = unpk(q1), u2 = unpk(q2), u3 = unpk(q3);
    PIN(u0); PIN(u1); PIN(u2); PIN(u3);
    SCHED();
    settle4(q4, q5, q6, q7, tag, vo, dws, sb);
    ST(4);
    u0 += unpk(q4);
    u1 += unpk(q5);
    u2 += unpk(q6);
    u3 += unpk(q7);
    *(LAS v2u*)(L + qfw) = v2u{pk(u0[0], u0[1]), pk(u0[2], u0[3])};
    *(LAS v2u*)(L + qfw + 512) = v2u{pk(u1[0], u1[1]), pk(u1[2], u1[3])};
    *(LAS v2u*)(L + qfw + RS) = v2u{pk(u2[0], u2[1]), pk(u2[2], u2[3])};
    *(LAS v2u*)(L + qfw + RS + 512) = v2u{pk(u3[0], u3[1]), pk(u3[2], u3[3])};
  }
  ST(5);
  bar();
  f4 sa = {0.f, 0.f, 0.f, 0.f};
  v4u wv[5];
  const v3u xd = __builtin_amdgcn_raw_buffer_load_b96(mk(x, 2560), ((n >> 3) * 1280 + frow * 2) & ~3, 0, 0);
  {
    v4u qf[16];
#pragma unroll
    for (int s = 0; s < 16; ++s) qf[s] = *(LAS v4u*)(L + qfr + s * 64);
    SCHED();
#pragma unroll
    for (int s = 0; s < 16; ++s) {
      sa = mfma(kf[s], qf[s], sa);
      if (s % 3 == 0 && s < 15) {
        wv[s / 3] = *(const GAS v4u*)((s < 9 ? wp : wp2) + ((s / 3) % 3 - 1) * 3456);
        SCHED();
      }
    }
  }
  {
    float m = fmaxf(fmaxf(sa[0], sa[1]), fmaxf(sa[2], sa[3]));
    auto s1 = __builtin_amdgcn_permlane16_swap(__float_as_uint(m), __float_as_uint(m), false, false);
    m = max2(__uint_as_float(s1[0]), __uint_as_float(s1[1]));
    *(LAS float*)(L + mxw) = m;
  }
  ST(6);
  bar();
  float M;
  {
    const f4 m0 = *(LAS f4*)(L + mxr), m1 = *(LAS f4*)(L + mxr + 16), m2 = *(LAS f4*)(L + mxr + 32), m3 = *(LAS f4*)(L + mxr + 48);
    M = max3(max3(max3(m0[0], m0[1], m0[2]), max3(m0[3], m1[0], m1[1]), max3(m1[2], m1[3], m2[0])), max3(m2[1], m2[2], m2[3]),
             max2(max3(m3[0], m3[1], m3[2]), m3[3]));
    const u32 p01 = pk(__builtin_amdgcn_exp2f(sa[0] - M), __builtin_amdgcn_exp2f(sa[1] - M));
    const u32 p23 = pk(__builtin_amdgcn_exp2f(sa[2] - M), __builtin_amdgcn_exp2f(sa[3] - M));
    *(LAS v2u*)(L + pfw) = v2u{p01, p23};
  }
  bar();
  ST(7);
  {
    v4u pf[4];
#pragma unroll
    for (int s = 0; s < 4; ++s) pf[s] = *(LAS v4u*)(L + pfr + s * 1024);
    SCHED();
    if (w == 0) {
      const v4u one = {0x3F803F80u, 0x3F803F80u, 0x3F803F80u, 0x3F803F80u};
      f4 l = {0.f, 0.f, 0.f, 0.f};
#pragma unroll
      for (int s = 0; s < 4; ++s) l = mfma(one, pf[s], l);
      stc(v4u{__float_as_uint(M), tag, __float_as_uint(l[0]), tag}, rws, mlo);
    }
    SCHED();
    f4 o[4];
#pragma unroll
    for (int t = 0; t < 4; ++t) o[t] = f4{0.f, 0.f, 0.f, 0.f};
#pragma unroll
    for (int s = 0; s < 4; ++s)
#pragma unroll
      for (int t = 0; t < 4; ++t) o[t] = mfma(pf[s], vf[s * 4 + t], o[t]);
    u32 pa[4], pb[4];
#pragma unroll
    for (int k = 0; k < 4; ++k) {
      pa[k] = pk(o[0][k], o[1][k]);
      pb[k] = pk(o[2][k], o[3][k]);
    }
#pragma unroll
    for (int k = 0; k < 2; ++k) {
      v4u pc;
      pc.x = __builtin_amdgcn_update_dpp(pa[k], pa[2 + k], ROR8, 0xf, 0xc, false);
      pc.y = __builtin_amdgcn_update_dpp(pb[k], pb[2 + k], ROR8, 0xf, 0xc, false);
      pc.z = __builtin_amdgcn_update_dpp(pa[2 + k], pa[k], ROR8, 0xf, 0x3, false);
      pc.w = __builtin_amdgcn_update_dpp(pb[2 + k], pb[k], ROR8, 0xf, 0x3, false);
      stc(pc, rws, k ? o_v1 : o_vo);
    }
  }
  ST(8);
  if (u < 108) {
    bar0();
    v4u dv[4];
    {
      const rsrc_t rd = mk(wd, 1728 * 1280);
      const int so = w < 5 ? u * 20480 + w * 256 : OOB;
#pragma unroll
      for (int t = 0; t < 4; ++t) dv[t] = __builtin_amdgcn_raw_buffer_load_b128(rd, g * 4 * 1280 + n * 16 + t * 1280, so, 0);
      SCHED();
    }
    const v4u sm = selmask(n);
    const u32 sh = (g & 1) << 4;
    const u32 a0 = __builtin_amdgcn_alignbit(xd.y, xd.x, sh), a1 = __builtin_amdgcn_alignbit(xd.z, xd.y, sh), a2 = xd.z >> sh;
    f4 acc = mfma(sel(duplo(a0), sm), wv[0], f4{0.f, 0.f, 0.f, 0.f});
    acc = mfma(sel(duphi(a0), sm), wv[1], acc);
    acc = mfma(sel(duplo(a1), sm), wv[2], acc);
    acc = mfma(sel(duphi(a1), sm), wv[3], acc);
    acc = mfma(sel(duplo(a2), sm), wv[4], acc);
    *(LAS f4*)(L + L2_QF + w * 1024 + lane * 16) = acc;
    bar();
    if (w == 6) *(LAS float*)(L + L2_MX + (lane >> 5) * 4) = 1.f - mix_gate(x, gw, lane);
    if (w == 7) {
      f4 e[8];
#pragma unroll
      for (int k = 0; k < 8; ++k) e[k] = *(LAS f4*)(L + L2_QF + k * 1024 + lane * 16);
      SCHED();
      f4 s = ((e[0] + e[1]) + (e[2] + e[3])) + ((e[4] + e[5]) + (e[6] + e[7]));
      ST(9);
      s = exchange4(s, rws, dws, u, lane, tag);
      float hv[4];
#pragma unroll
      for (int k = 0; k < 4; ++k) hv[k] = s[k] * dpp<ROR8>(s[k]) * __builtin_amdgcn_rcpf(1.f + __expf(-s[k]));
      *(LAS v2u*)(L + L2_H + (g >> 1) * 256 + n * 16 + (g & 1) * 8) = v2u{pk(hv[0], hv[1]), pk(hv[2], hv[3])};
      ST(10);
    }
    int hr = L2_H + (n >> 3) * 256 + (j & 3) * 32 + g * 8, fo = WS_FF + u * 5120 + w * 512 + (g >> 1) * 2560 + n * 32 + (g & 1) * 16;
    PIN(hr); PIN(fo);
    bar();
    if (w < 5) {
      const v2u hh = *(LAS v2u*)(L + hr);
      const float og = *(LAS float*)(L + L2_MX + (lane >> 5) * 4);
      f4 d = mfma(sel(duplo(hh.x), sm), dv[0], f4{0.f, 0.f, 0.f, 0.f});
      d = mfma(sel(duphi(hh.x), sm), dv[1], d);
      d = mfma(sel(duplo(hh.y), sm), dv[2], d);
      d = mfma(sel(duphi(hh.y), sm), dv[3], d);
      d *= og;
      stc(v4u{pk(d[0], d[1]), tag, pk(d[2], d[3]), tag}, rws, fo);
    }
    ST(11);
  }
  if (u >= 88) {
    const int T = u - 88;
    int v0, v3, st, so;
    if (w < 4) {
      so = WS_FA + T * 16384 + w * 4096;
      v0 = lane * 16;
      st = 1024;
      v3 = v0 + 3072;
    } else {
      so = WS_FF + T * 64 + (w - 4) * 27 * 5120;
      v0 = (lane >> 3) * 5120 + ((lane >> 2) & 1) * 2560 + (lane & 3) * 16;
      st = 8 * 5120;
      v3 = lane < 24 ? v0 + 24 * 5120 : v0;
    }
    int v1 = v0 + st, v2 = v1 + st;
    int fw = L2_FR + w * 128 + (lane & 7) * 16;
    int fr = L2_FR + n * 8 + (g & 1) * 4, fr2 = L2_FR + 512 + (g & 1) * 64 + n * 4;
    PIN(fr); PIN(fr2);
    fr = (lane & 32) ? fr2 : fr;
    int ovo = ((lane & 32) << 25) | ((g & 1) * 1280 + n * 2 + T * 32);
    u32 keep = (w >= 4 && lane >= 24) ? 0u : ~0u;
    const u32 o1 = 0x3F80u << ((n & 1) << 4);
    v4u pick = {(n & 2) ? 0u : o1, 0u, (n & 2) ? o1 : 0u, 0u};
    PIN(v0); PIN(v1); PIN(v2); PIN(v3); PIN(fw); PIN(fr); PIN(ovo); PIN(pick); PIN(keep);
    const rsrc_t ro = mk(out, 2560);
    if (FA_REP) {
      const int rso = HEADSUP && w < 4 ? WS_HU + (32 * w + (T & 31)) * 128 : so;
      v4u a = ldc(rws, 0, rso);
#pragma unroll 1
      for (u32 it = 0; it < SPIN_CAP; ++it) {
        if (!tagbad(a, tag)) break;
        reload1(a, ~0ull, 0, dws, rso);
      }
    }
    ST(12);
    asm volatile("" ::: "memory");
    v4u a0 = ldc(rws, v0, so), a1 = ldc(rws, v1, so), a2 = ldc(rws, v2, so), a3 = ldc(rws, v3, so);
    if (__builtin_expect(!alltag(tag, a0, a1, a2, a3), 0)) {
#pragma unroll 1
      for (u32 it = 0; it < SPIN_CAP; ++it) {
        const u64 m0 = tagbad(a0, tag), m1 = tagbad(a1, tag), m2 = tagbad(a2, tag), m3 = tagbad(a3, tag);
        if (!(m0 | m1 | m2 | m3)) break;
        reload4v(a0, a1, a2, a3, m0, m1, m2, m3, v0, v1, v2, v3, dws, so);
      }
    }
    ST(13);
    a3.x &= keep;
    a3.z &= keep;
    f4 v = mfma(pick, a0, f4{0.f, 0.f, 0.f, 0.f});
    v = mfma(pick, a1, v);
    v = mfma(pick, a2, v);
    v = mfma(pick, a3, v);
#pragma unroll
    for (int k = 0; k < 4; ++k) v[k] += dpp<ROR8>(v[k]);
    *(LAS f4*)(L + fw) = v;
    bar();
    if (w == 0) {
      float A = (*(LAS float*)(L + fr) + *(LAS float*)(L + fr + 128)) + (*(LAS float*)(L + fr + 256) + *(LAS float*)(L + fr + 384));
      auto t = __builtin_amdgcn_permlane32_swap(__float_as_uint(A), __float_as_uint(A), false, false);
      A = __uint_as_float(t[0]) + __uint_as_float(t[1]);
      __builtin_amdgcn_raw_buffer_store_b16((u16)pk(A, A), ro, ovo, 0, 0);
      if (T == 0) stc1(epoch, rws, lane == 0 ? WS_EPOCH : OOB, 0);
    }
    ST(14);
  }
  if (bid == 197 && w == 0) { const u64 e0 = __builtin_amdgcn_s_memrealtime(); while (__builtin_amdgcn_s_memrealtime() < e0 + lat_) __builtin_amdgcn_s_sleep(1); }
  STEND();
}

extern "C" void kernel_launch(void* const* d_in, const int* in_sizes, int n_in, void* d_out, int out_size, void* d_ws, size_t ws_size,
                              hipStream_t stream) {
  mla_ffn_mix<<<256, 512, 0, stream>>>((const u16*)d_in[0], (const u16*)d_in[1], (const u16*)d_in[2], (const u16*)d_in[3],
                                       (const u16*)d_in[4], (const u16*)d_in[5], (const u16*)d_in[7], (const u16*)d_in[8],
                                       (const u16*)d_in[9], (const u16*)d_in[10], (u16*)d_out, (unsigned char*)d_ws);
}
```

```cpp
#define CODE_BYTES 15104
#include <hip/hip_runtime.h>
#include <stdint.h>

typedef unsigned u32;
typedef unsigned long long u64;
typedef unsigned short u16;
typedef u32 v4u __attribute__((ext_vector_type(4)));
typedef u32 v3u __attribute__((ext_vector_type(3)));
typedef u32 v2u __attribute__((ext_vector_type(2)));
typedef float f4 __attribute__((ext_vector_type(4)));
typedef __bf16 bf8 __attribute__((ext_vector_type(8)));
typedef __bf16 bf2 __attribute__((ext_vector_type(2)));
typedef float f2 __attribute__((ext_vector_type(2)));
typedef short s4 __attribute__((ext_vector_type(4)));
typedef short s2 __attribute__((ext_vector_type(2)));
#define GAS __attribute__((address_space(1)))
#define LAS __attribute__((address_space(3)))
typedef __amdgpu_buffer_rsrc_t rsrc_t;

constexpr int AUX_SC1 = 16;
#define POL ""
#define G2_DELAY 0
#define G1_DELAY 0
#define QG_DELAY 0
#define FA_REP 1
#define BADCOL 1
#define PROBE_THR 130
#define UK_LAST 1
#define HMAP 1
#define CLEAN_DELAY 0
#define CKV_ORDER 2
#define QA_TOUCH 0
#define FFN_TOUCH 0
#define HEADSUP 0
#define USE_NT 1
#define HELP_UK 1
#define HELP_KV 0
#define QT_DELAY 16
#define GU_EARLY 3
#define UK_DELAY 16
#define CLEAN_POS 1
#define WD_POS 0
#define VF_EARLY 1
#define EP_EARLY 1
#define BAD_EARLY 1
#define OUT_AUX 0
#define PRE_FA 1
#define RED_MAP 0
#define PR_DELAY 4
#define WD_EARLY 0
#define KV_WAIT 0
#define DMA4 0
#define CMIX 1
#define PACE 63
constexpr u32 SPIN_CAP = 1u << 16;
constexpr u32 SENT = 0xFFFFFFFFu;
constexpr int OOB = 0x40000000;

constexpr int WS_EPOCH = 0;
#define P0 0
#define QNUMA 0
#define P1 0
constexpr int WS_VT = 8192;
constexpr int WS_PR = 0x1F0000;
constexpr int WS_F1 = 16384, WS_HU = WS_F1;
constexpr int WS_ML = 32768;
constexpr int WS_QP = 65536;
constexpr int WS_QA = WS_QP + 128 * 1024;
constexpr int WS_FA = WS_QA + 128 * 1024;
constexpr int WS_OB = 2 * 1024 * 1024;
constexpr int WS_FF = 4 * 1024 * 1024;
constexpr int WS_END = 6 * 1024 * 1024;
static_assert(WS_FA + 40 * 32768 <= WS_OB, "");
__device__ __forceinline__ int ob_row(int b, int h) { return WS_OB + (b * 16 + (h & 3) + ((h >> 3) << 2)) * 65536 + ((((h >> 2) ^ P1) & 1) << 13); }
__device__ __forceinline__ int ob_kg(int kg) { return (kg >> 3) * 16384 + (kg & 7) * 1024; }
__device__ __forceinline__ int qa_row(int dh, int b, int h) { return WS_QA + (dh * 4 + (h >> 3)) * 16384 + (((b ^ (QNUMA ? P0 : dh)) & 1) << 13) + (h & 7) * 1024; }
__device__ __forceinline__ int ml_bk(int bk) { return (bk >> 5) * 16384 + (bk & 31) * 256; }
__device__ __forceinline__ int ml_h(int h) { return WS_ML + ((((h >> 2) ^ P0) & 1) << 13) + ((h & 3) + ((h >> 3) << 2)) * 16; }
constexpr int FPAR = ((1 ^ P0) & 1) << 13;

constexpr int RS = 1056;
constexpr int L2_QF = 128 * RS;
constexpr int L2_PF = L2_QF + 16 * RS;
constexpr int L2_MX = L2_PF + 4096;
constexpr int L2_FR = L2_MX + 1280;
constexpr int L2_H = L2_FR + 1024;
constexpr int L_DUMP = L2_H + 512;
constexpr int LDS_BYTES = L_DUMP + 512;
constexpr int L1_UV = 0, L1_WO = 32768, L1_X = L1_WO + 81920, L1_LA = L1_X + 8192, L1_CX = L1_LA + 4 * 528, L1_Q = L1_CX + 4 * 144,
              L1_W = L1_Q + 16 * 144, L1_H = L1_W + 128;
static_assert(L1_H + 512 <= L_DUMP && LDS_BYTES <= 163840, "");

__device__ __forceinline__ f4 mfma(v4u a, v4u b, f4 c) {
  return __builtin_amdgcn_mfma_f32_16x16x32_bf16(__builtin_bit_cast(bf8, a), __builtin_bit_cast(bf8, b), c, 0, 0, 0);
}
__device__ __forceinline__ f4 mfma16(v2u a, v2u b, f4 c) {
  return __builtin_amdgcn_mfma_f32_16x16x16bf16_1k(__builtin_bit_cast(s4, a), __builtin_bit_cast(s4, b), c, 0, 0, 0);
}
__device__ __forceinline__ u32 pk(float lo, float hi) {
  f2 v = {lo, hi};
  return __builtin_bit_cast(u32, __builtin_convertvector(v, bf2));
}
__device__ __forceinline__ float blo(u32 v) { return __uint_as_float(v << 16); }
__device__ __forceinline__ float bhi(u32 v) { return __uint_as_float(v & 0xFFFF0000u); }
template <int CTRL> __device__ __forceinline__ float dpp(float x) {
  return __builtin_bit_cast(float, __builtin_amdgcn_mov_dpp(__builtin_bit_cast(int, x), CTRL, 0xf, 0xf, true));
}
constexpr int XOR1 = 0xB1, XOR2 = 0x4E, ROR8 = 0x128, ROR4 = 0x124;
__device__ __forceinline__ float max3(float a, float b, float c) {
  float r;
  asm("v_max3_f32 %0, %1, %2, %3" : "=v"(r) : "v"(a), "v"(b), "v"(c));
  return r;
}
__device__ __forceinline__ float max2(float a, float b) {
  float r;
  asm("v_max_f32 %0, %1, %2" : "=v"(r) : "v"(a), "v"(b));
  return r;
}
__device__ __forceinline__ float x16_max(float x) {
  auto s = __builtin_amdgcn_permlane16_swap(__float_as_uint(x), __float_as_uint(x), false, false);
  return fmaxf(__uint_as_float(s[0]), __uint_as_float(s[1]));
}
__device__ __forceinline__ float x16_sum(float x) {
  auto s = __builtin_amdgcn_permlane16_swap(__float_as_uint(x), __float_as_uint(x), false, false);
  return __uint_as_float(s[0]) + __uint_as_float(s[1]);
}
__device__ __forceinline__ float x32_max(float x) {
  auto t = __builtin_amdgcn_permlane32_swap(__float_as_uint(x), __float_as_uint(x), false, false);
  return fmaxf(__uint_as_float(t[0]), __uint_as_float(t[1]));
}
__device__ __forceinline__ rsrc_t mk(const void* p, u32 bytes) { return __builtin_amdgcn_make_buffer_rsrc((void*)p, 0, bytes, 0x00020000); }
__device__ __forceinline__ v4u mkd(const void* p, u32 bytes) {
  return v4u{(u32)(size_t)p, (u32)((size_t)p >> 32) & 0xFFFFu, bytes, 0x00020000u};
}
__device__ __forceinline__ v4u ldc(rsrc_t r, int vo, int so) { return __builtin_amdgcn_raw_buffer_load_b128(r, vo, so, AUX_SC1); }
__device__ __forceinline__ void stc(v4u v, rsrc_t r, int vo) { __builtin_amdgcn_raw_buffer_store_b128(v, r, vo, 0, AUX_SC1); }
__device__ __forceinline__ void stc2(v2u v, rsrc_t r, int vo, int so) { __builtin_amdgcn_raw_buffer_store_b64(v, r, vo, so, AUX_SC1); }
__device__ __forceinline__ void stc1(u32 v, rsrc_t r, int vo, int so) { __builtin_amdgcn_raw_buffer_store_b32(v, r, vo, so, AUX_SC1); }
template <int N, int SSTEP, int LSTEP, int KEEP = 63, int NT = 0> __device__ __forceinline__ void dma_run(v4u d, int vo, int so, int lds) {
  int t;
  asm volatile("s_mov_b32 m0, %[l]\n\ts_mov_b32 %[t], %[s]\n\t.rept %[n]\n\t.if %[nt]\n\tbuffer_load_dwordx4 %[v], %[d], %[t] offen nt lds\n\t.else\n\t"
               "buffer_load_dwordx4 %[v], %[d], %[t] offen lds\n\t.endif\n\t"
               "s_add_u32 m0, m0, %[ls]\n\ts_add_u32 %[t], %[t], %[ss]\n\t.if %[k] < 63\n\ts_waitcnt vmcnt(%[k])\n\t.endif\n\t.endr"
               : [t] "=&s"(t)
               : [v] "v"(vo), [d] "s"(d), [s] "s"(so), [l] "s"(lds), [n] "i"(N), [ls] "i"(LSTEP), [ss] "i"(SSTEP), [k] "i"(KEEP), [nt] "i"(NT)
               : "memory", "scc");
}
template <int N, int SSTEP, int LSTEP, int NT = 0> __device__ __forceinline__ void dma_run2(v4u d, int va, int vb, int so, int lds) {
  int t;
  asm volatile("s_mov_b32 m0, %[l]\n\ts_mov_b32 %[t], %[s]\n\t.rept %[n]\n\t.if %[nt]\n\tbuffer_load_dwordx4 %[va], %[d], %[t] offen nt lds\n\t"
               "s_add_u32 m0, m0, 0x400\n\ts_nop 0\n\tbuffer_load_dwordx4 %[vb], %[d], %[t] offen nt lds\n\t.else\n\t"
               "buffer_load_dwordx4 %[va], %[d], %[t] offen lds\n\t"
               "s_add_u32 m0, m0, 0x400\n\ts_nop 0\n\tbuffer_load_dwordx4 %[vb], %[d], %[t] offen lds\n\t.endif\n\t"
               "s_add_u32 m0, m0, %[ls]\n\ts_add_u32 %[t], %[t], %[ss]\n\t.endr"
               : [t] "=&s"(t)
               : [va] "v"(va), [vb] "v"(vb), [d] "s"(d), [s] "s"(so), [l] "s"(lds), [n] "i"(N), [ls] "i"(LSTEP - 1024), [ss] "i"(SSTEP), [nt] "i"(NT)
               : "memory", "scc");
}
template <int N, int SSTEP, int LSTEP, int LD> __device__ __forceinline__ void dma_zip(v4u d, int vo, int sa, int sb, int lds) {
  int t, t2;
  asm volatile("s_mov_b32 m0, %[l]\n\ts_mov_b32 %[t], %[sa]\n\ts_mov_b32 %[u], %[sb]\n\t.rept %[n]\n\t"
               "buffer_load_dwordx4 %[v], %[d], %[t] offen lds\n\ts_add_u32 m0, m0, %[ld]\n\ts_add_u32 %[t], %[t], %[ss]\n\t"
               "buffer_load_dwordx4 %[v], %[d], %[u] offen lds\n\ts_sub_u32 m0, m0, %[lb]\n\ts_add_u32 %[u], %[u], %[ss]\n\t.endr"
               : [t] "=&s"(t), [u] "=&s"(t2)
               : [v] "v"(vo), [d] "s"(d), [sa] "s"(sa), [sb] "s"(sb), [l] "s"(lds), [n] "i"(N), [ld] "i"(LD), [lb] "i"(LD - LSTEP), [ss] "i"(SSTEP)
               : "memory", "scc");
}
__device__ __forceinline__ void touch(v4u d, int vo, int so, int lds) {
  asm volatile("s_mov_b32 m0, %[l]\n\ts_nop 0\n\tbuffer_load_dword %[v], %[d], %[s] offen lds" ::[v] "v"(vo), [d] "s"(d), [s] "s"(so), [l] "s"(lds)
               : "memory");
}
__device__ __forceinline__ v2u trd(LAS unsigned char* p) {
  return __builtin_bit_cast(v2u, __builtin_amdgcn_ds_read_tr16_b64_v4i16((LAS s4*)p));
}
__device__ __forceinline__ v4u cat(v2u a, v2u b) { return v4u{a.x, a.y, b.x, b.y}; }
#define SCHED() __builtin_amdgcn_sched_barrier(0)
#define PIN(x) asm volatile("" : "+v"(x))
#define PINS(x) asm volatile("" : "+s"(x))
__device__ __forceinline__ void bar() {
  asm volatile("s_waitcnt lgkmcnt(0)" ::: "memory");
  __builtin_amdgcn_s_barrier();
  asm volatile("" ::: "memory");
}
__device__ __forceinline__ void bar0() {
  SCHED();
  asm volatile("" ::: "memory");
  __builtin_amdgcn_s_barrier();
  asm volatile("" ::: "memory");
  SCHED();
}
#define VMW(n) asm volatile("s_waitcnt vmcnt(" #n ")" ::: "memory")
#define DRAIN() VMW(0)
__device__ __forceinline__ u64 tagbad(v4u a, u32 tag) { return __builtin_amdgcn_ballot_w64(a.y != tag) | __builtin_amdgcn_ballot_w64(a.w != tag); }
__device__ __forceinline__ u64 sentbad(v4u a) { return __builtin_amdgcn_ballot_w64(max(max(a.x, a.y), max(a.z, a.w)) == SENT); }
#define CX(i) "v_cmpx_eq_u32 vcc, %[t], %[a" #i "]\n\t"
__device__ __forceinline__ bool alltag(u32 tag, v4u p, v4u q, v4u r, v4u s) {
  u64 e;
  asm volatile(CX(0) CX(1) CX(2) CX(3) CX(4) CX(5) CX(6) CX(7) "s_mov_b64 %[e], exec\n\ts_mov_b64 exec, -1\n\ts_nop 1"
               : [e] "=s"(e)
               : [t] "s"(tag), [a0] "v"(p.y), [a1] "v"(p.w), [a2] "v"(q.y), [a3] "v"(q.w), [a4] "v"(r.y), [a5] "v"(r.w), [a6] "v"(s.y), [a7] "v"(s.w)
               : "vcc");
  return e == ~0ull;
}
#define RL1(i) "s_mov_b64 exec, %[m" #i "]\n\ts_cbranch_execz " #i "f\n\tbuffer_load_dwordx4 %[a" #i "], %[v" #i "], %[rs], %[so] offen offset:%[o" #i "] sc1\n" #i ":\n\t"
__device__ __forceinline__ void reload4(v4u& a1, v4u& a2, v4u& a3, v4u& a4, u64 m1, u64 m2, u64 m3, u64 m4, int v, v4u rs, int so) {
  asm volatile("s_nop 4\n\t" RL1(1) RL1(2) RL1(3) RL1(4) "s_mov_b64 exec, -1\n\ts_waitcnt vmcnt(0)"
               : [a1] "+v"(a1), [a2] "+v"(a2), [a3] "+v"(a3), [a4] "+v"(a4)
               : [m1] "s"(m1), [m2] "s"(m2), [m3] "s"(m3), [m4] "s"(m4), [v1] "v"(v), [v2] "v"(v), [v3] "v"(v), [v4] "v"(v), [rs] "s"(rs),
                 [so] "s"(so), [o1] "i"(0), [o2] "i"(1024), [o3] "i"(2048), [o4] "i"(3072)
               : "memory");
}
__device__ __forceinline__ void reload3(v4u& a1, v4u& a2, v4u& a3, u64 m1, u64 m2, u64 m3, int v1, int v2, int v3, v4u rs, int so) {
  asm volatile("s_nop 4\n\t" RL1(1) RL1(2) RL1(3) "s_mov_b64 exec, -1\n\ts_waitcnt vmcnt(0)"
               : [a1] "+v"(a1), [a2] "+v"(a2), [a3] "+v"(a3)
               : [m1] "s"(m1), [m2] "s"(m2), [m3] "s"(m3), [v1] "v"(v1), [v2] "v"(v2), [v3] "v"(v3), [rs] "s"(rs), [so] "s"(so), [o1] "i"(0),
                 [o2] "i"(0), [o3] "i"(0)
               : "memory");
}
__device__ __forceinline__ void reload1(v4u& a1, u64 m1, int v1, v4u rs, int so) {
  asm volatile("s_nop 4\n\t" RL1(1) "s_mov_b64 exec, -1\n\ts_waitcnt vmcnt(0)"
               : [a1] "+v"(a1)
               : [m1] "s"(m1), [v1] "v"(v1), [rs] "s"(rs), [so] "s"(so), [o1] "i"(0)
               : "memory");
}
__device__ __forceinline__ void reload4v(v4u& a1, v4u& a2, v4u& a3, v4u& a4, u64 m1, u64 m2, u64 m3, u64 m4, int v1, int v2, int v3, int v4, v4u rs,
                                         int so) {
  asm volatile("s_nop 4\n\t" RL1(1) RL1(2) RL1(3) RL1(4) "s_mov_b64 exec, -1\n\ts_waitcnt vmcnt(0)"
               : [a1] "+v"(a1), [a2] "+v"(a2), [a3] "+v"(a3), [a4] "+v"(a4)
               : [m1] "s"(m1), [m2] "s"(m2), [m3] "s"(m3), [m4] "s"(m4), [v1] "v"(v1), [v2] "v"(v2), [v3] "v"(v3), [v4] "v"(v4), [rs] "s"(rs),
                 [so] "s"(so), [o1] "i"(0), [o2] "i"(0), [o3] "i"(0), [o4] "i"(0)
               : "memory");
}
__device__ __forceinline__ void settle4(v4u& a, v4u& b, v4u& c, v4u& d, u32 tag, int v, v4u rs, int so) {
  if (__builtin_expect(!alltag(tag, a, b, c, d), 0)) {
#pragma unroll 1
    for (u32 it = 0; it < SPIN_CAP; ++it) {
      const u64 m0 = tagbad(a, tag), m1 = tagbad(b, tag), m2 = tagbad(c, tag), m3 = tagbad(d, tag);
      if (!(m0 | m1 | m2 | m3)) break;
      __builtin_amdgcn_s_sleep(2);
      reload4(a, b, c, d, m0, m1, m2, m3, v, rs, so);
    }
  }
}
#define RL3(i, so, o) "s_mov_b64 exec, %[m" #i "]\n\ts_cbranch_execz " #i "f\n\tbuffer_load_dwordx4 %[a" #i "], %[v], %[rs], %[" #so "] offen offset:" #o " sc1\n" #i ":\n\t"
__device__ __forceinline__ bool anysent(v4u a, v4u b, v4u c, v4u d) {
  const u32 ma = max(max(max(max(a.x, a.y), a.z), max(max(a.w, b.x), b.y)), max(b.z, b.w));
  const u32 mb = max(max(max(max(c.x, c.y), c.z), max(max(c.w, d.x), d.y)), max(d.z, d.w));
  return __builtin_amdgcn_ballot_w64(max(ma, mb) == SENT) != 0ull;
}
__device__ __forceinline__ void settle22(v4u& a, v4u& b, v4u& c, v4u& d, int v, v4u rs, int sa, int sb) {
  if (__builtin_expect(anysent(a, b, c, d), 0)) {
#pragma unroll 1
    for (u32 it = 0; it < SPIN_CAP; ++it) {
      const u64 m1 = sentbad(a), m2 = sentbad(b), m3 = sentbad(c), m4 = sentbad(d);
      if (!(m1 | m2 | m3 | m4)) break;
      __builtin_amdgcn_s_sleep(2);
      asm volatile("s_nop 4\n\t" RL3(1, sa, 0) RL3(2, sa, 1024) RL3(3, sb, 0) RL3(4, sb, 1024) "s_mov_b64 exec, -1\n\ts_waitcnt vmcnt(0)"
                   : [a1] "+v"(a), [a2] "+v"(b), [a3] "+v"(c), [a4] "+v"(d)
                   : [m1] "s"(m1), [m2] "s"(m2), [m3] "s"(m3), [m4] "s"(m4), [v] "v"(v), [rs] "s"(rs), [sa] "s"(sa), [sb] "s"(sb)
                   : "memory");
    }
  }
}
__device__ __forceinline__ void wait_flags(u32 f, u32 tag, rsrc_t rws, int lane, int so) {
#pragma unroll 1
  for (u32 it = 0; it < SPIN_CAP; ++it) {
    if (__builtin_amdgcn_ballot_w64(f != tag) == 0ull) break;
    __builtin_amdgcn_s_sleep(4);
    asm volatile("" ::: "memory");
    f = __builtin_amdgcn_raw_buffer_load_b32(rws, lane * 128, so, AUX_SC1);
  }
}
__device__ __forceinline__ u32 count_votes(v2u f, u32 tag, rsrc_t rws, int lane) {
#pragma unroll 1
  for (u32 it = 0; it < SPIN_CAP; ++it) {
    if ((__builtin_amdgcn_ballot_w64(f.y != tag) & 0xFFFFFFFFFFFFull) == 0ull) break;
    __builtin_amdgcn_s_sleep(4);
    asm volatile("" ::: "memory");
    f = __builtin_amdgcn_raw_buffer_load_b64(rws, lane < 48 ? lane * 128 : OOB, WS_VT, AUX_SC1);
  }
  u32 m = f.x;
  m = min(m, (u32)__builtin_amdgcn_mov_dpp(m, XOR1, 0xf, 0xf, true));
  m = min(m, (u32)__builtin_amdgcn_mov_dpp(m, XOR2, 0xf, 0xf, true));
  m = min(m, (u32)__builtin_amdgcn_mov_dpp(m, ROR4, 0xf, 0xf, true));
  m = min(m, (u32)__builtin_amdgcn_mov_dpp(m, ROR8, 0xf, 0xf, true));
  const u64 k = __builtin_amdgcn_ballot_w64((m & 1u) != 0u);
  return ((u32)k & 0x2000u) | (((u32)k >> 16) & 0x4000u) | ((u32)(k >> 32) & 0x8000u);
}
__device__ __forceinline__ u32 addbf(u32 a, u32 b) { return pk(blo(a) + blo(b), bhi(a) + bhi(b)); }
__device__ __forceinline__ v4u sel(u32 dup, v4u m) { return v4u{dup & m.x, dup & m.y, dup & m.z, dup & m.w}; }
__device__ __forceinline__ v4u selmask(int n) {
  const u32 v = 0xFFFFu << ((n & 1) << 4);
  const int q = (n >> 1) & 3;
  return v4u{q == 0 ? v : 0u, q == 1 ? v : 0u, q == 2 ? v : 0u, q == 3 ? v : 0u};
}
__device__ __forceinline__ u32 duplo(u32 d) { return __builtin_amdgcn_perm(d, d, 0x01000100u); }
__device__ __forceinline__ u32 duphi(u32 d) { return __builtin_amdgcn_perm(d, d, 0x03020302u); }
__device__ __forceinline__ f4 unpk(v4u a) { return f4{blo(a.x), bhi(a.x), blo(a.z), bhi(a.z)}; }

__device__ __forceinline__ f4 exchange4(f4 s, rsrc_t rws, v4u dws, int so, int q, int lane, u32 tag) {
  const int v0 = q * 1024 + lane * 16;
  stc(v4u{pk(s[0], s[1]), tag, pk(s[2], s[3]), tag}, rws, so + v0);
  DRAIN();
  v4u q1 = ldc(rws, v0 ^ 1024, so), q2 = ldc(rws, v0 ^ 2048, so), q3 = ldc(rws, v0 ^ 3072, so);
#pragma unroll 1
  for (u32 it = 0; it < SPIN_CAP; ++it) {
    const u64 m1 = tagbad(q1, tag), m2 = tagbad(q2, tag), m3 = tagbad(q3, tag);
    if (!(m1 | m2 | m3)) break;
    __builtin_amdgcn_s_sleep(2);
    reload3(q1, q2, q3, m1, m2, m3, v0 ^ 1024, v0 ^ 2048, v0 ^ 3072, dws, so);
  }
  return (s + unpk(q1)) + (unpk(q2) + unpk(q3));
}

__device__ __forceinline__ void gate_load(v2u (&a)[5], v2u (&c)[5], const u16* x, const u16* gw, int lane) {
  const rsrc_t rx = mk(x, 2560), rg = mk(gw, 1280);
#pragma unroll
  for (int k = 0; k < 5; ++k) {
    a[k] = __builtin_amdgcn_raw_buffer_load_b64(rx, (lane >> 5) * 1280 + (lane & 31) * 8 + k * 256, 0, 0);
    c[k] = __builtin_amdgcn_raw_buffer_load_b64(rg, (lane & 31) * 8 + k * 256, 0, 0);
  }
}
__device__ __forceinline__ float mix_gate(const v2u (&a)[5], const v2u (&c)[5]) {
  float s = 0.f;
#pragma unroll
  for (int k = 0; k < 5; ++k) s += (blo(a[k].x) * blo(c[k].x) + bhi(a[k].x) * bhi(c[k].x)) + (blo(a[k].y) * blo(c[k].y) + bhi(a[k].y) * bhi(c[k].y));
  s += dpp<XOR1>(s);
  s += dpp<XOR2>(s);
  s += dpp<ROR4>(s);
  s += dpp<ROR8>(s);
  return 1.f / (1.f + __expf(-x16_sum(s)));
}

#define ST(i)
#define STEND()

extern "C" __global__ void __launch_bounds__(512, 2)
mla_ffn_mix(const u16* __restrict__ x, const u16* __restrict__ ckv, const u16* __restrict__ wq, const u16* __restrict__ wuk,
            const u16* __restrict__ wuv, const u16* __restrict__ wo, const u16* __restrict__ wg, const u16* __restrict__ wu,
            const u16* __restrict__ wd, const u16* __restrict__ gw, u16* __restrict__ out, unsigned char* __restrict__ ws) {
  __shared__ __attribute__((aligned(16))) unsigned char smem[LDS_BYTES];
  LAS unsigned char* L = (LAS unsigned char*)smem;
  const int LB = (int)(u32)(size_t)L;
  ST(0);
  const int tid = threadIdx.x, bid = blockIdx.x;
  {
    const u32 off = min((u32)(((bid & 0x38) * 2 + (tid >> 2)) * 128u), (u32)(CODE_BYTES - 128));
    u64 pc;
    asm volatile(
        "s_getpc_b64 %0\n\t"
        "v_readfirstlane_b32 vcc_lo, %1\n\t"
        "s_add_u32 vcc_lo, %3, vcc_lo\n\t"
        "s_lshr_b32 vcc_lo, vcc_lo, 6\n\t"
        "s_cmp_lg_u32 vcc_lo, 2\n\t"
        "s_cbranch_scc1 1f\n\t"
        "s_andn2_b64 %0, %0, 0xff\n\t"
        "s_mov_b32 m0, %4\n\t"
        "s_nop 0\n\t"
        "global_load_lds_dword %2, %0\n"
        "1:"
        : "=&s"(pc)
        : "v"(tid * 4), "v"(off), "s"(bid), "s"(LB + L_DUMP)
        : "memory", "scc", "vcc");
    SCHED();
  }
  const int lane = tid & 63, w = __builtin_amdgcn_readfirstlane(tid >> 6);
  const int xcd = bid & 7, r = bid >> 3;
  const int n = lane & 15, g = lane >> 4;
  const int h = (HMAP ? (xcd & 3) * 8 + (xcd & 4) : 4 * xcd) + (r >> 2), i = r & 3, dh = i >> 1, ch = i & 1;
  const int lr = lane >> 3, ckx = ((lane & 7) ^ (lr & 6)) << 4;
  const int col = h * 256 + dh * 128, r0 = 80 * (w ^ (4 * ch));
  int qva = ((lr >> 2) * 20 + (lr & 3)) * 8192 + ckx, qvb = qva + 40 * 8192, qso = r0 * 8192 + col, qld = LB + L1_WO + w * 10240;
  const int hcol = (HMAP ? (xcd & 3) * 8 + (xcd & 4) : 4 * xcd) * 256;
  int tvo = g * 8192 + (n >> 1) * 128, tso = (32 * (r - 16) + 4 * w) * 8192 + hcol;
  int bvo = lane * 8192, bso = 64 * (r - 16) * 8192 + hcol + 384 + w * 512;
  PIN(qva); PIN(qvb); PINS(qso); PINS(qld); PIN(tvo); PINS(tso); PIN(bvo); PINS(bso);
  SCHED();
  asm volatile("" ::"s"(x), "s"(ckv), "s"(wq), "s"(wuk), "s"(wuv), "s"(wo), "s"(wg), "s"(wu), "s"(wd), "s"(gw), "s"(out), "s"(ws));
  const v4u dq = mkd(wq, 640 * 8192);
  const v4u dk = mkd(wuk, 512 * 8192);
  if (bid < 128) {
    if (w < 4) dma_run2<5, 4 * 8192, 2048>(dq, qva, qvb, qso, qld);
  } else if (bid < 208 && w < (hcol == 7168 ? 2 : 1)) {
    touch(dq, bvo, bso, LB + L_DUMP);
  }
  SCHED();
  u32 epoch, tag;
  asm volatile("s_load_dword %0, %1, 0x0" : "=s"(epoch) : "s"(ws) : "memory");
#define GET_TAG()                                                                          \
  do {                                                                                     \
    asm volatile("s_waitcnt lgkmcnt(0)" : "+s"(epoch)::"memory");                          \
    epoch += 1u;                                                                           \
    tag = (epoch & 0x3FFFu) | ((epoch << 1) & 0x3FFF8000u) | ((epoch << 2) & 0x80000000u); \
    PINS(tag);                                                                             \
  } while (0)
  const rsrc_t rws = mk(ws, WS_END);
  const v4u dws = mkd(ws, WS_END);

  if (__builtin_expect(r < 16, 1)) {
    const int q = n >> 2, p = n & 3, sw = 2 * (g & 1) + (q >> 1), rb = (4 * g + q) * 128 + p * 8;
    v2u ga[5], gc[5];
    if (w == 0) gate_load(ga, gc, x, gw, lane);
    {
      int ta[4];
#pragma unroll
      for (int t = 0; t < 4; ++t) {
        ta[t] = L1_WO + w * 10240 + rb + ((t ^ sw) << 5);
        PIN(ta[t]);
      }
      int kvo = lr * 8192 + ckx, xvo = (lane >> 5) * 1280 + (lane & 31) * 16;
      PIN(kvo); PIN(xvo);
      const int qas = WS_QA + (dh * 4 + (h >> 3)) * 16384 + (h & 7) * 1024 + ch * 512;
      if (w == 7) {
        stc(v4u{SENT, SENT, SENT, SENT}, rws, qas + (lane >> 5) * 8192 + (lane & 31) * 16);
      }
      bar0();
      if (w >= 4) dma_run2<5, 4 * 8192, 2048>(dq, qva, qvb, qso, qld);
      dma_run<1, 0, 0>(mkd(x, 2560), xvo, r0 * 2, LB + L1_X + w * 1024);
      GET_TAG();
      int qw = L1_Q + (8 * (g >> 1) + w) * 144 + (g & 1) * 64 + n * 2, qr = L1_Q + n * 144 + g * 16;
      int svo = ((lane & 16) << 26) | ((((g >> 1) ^ (QNUMA ? P0 : dh)) & 1) * 8192 + n * 8), sso = qas + w * 128;
      int xr = L1_X + w * 1024 + (n >> 3) * 512 + g * 40, ur0 = L1_UV + w * 8192 + n * 512 + ((g ^ ((n & 1) << 2)) << 4);
      PIN(qw); PIN(qr); PIN(svo); PINS(sso); PIN(xr); PIN(ur0);
      if (w == 7) {
        VMW(11);
        stc1(tag, rws, lane == 0 ? WS_F1 + (h * 4 + i) * 128 : OOB, 0);
      }
      bar0();
      if (w < 4) dma_run<8, 8 * 8192, 1024>(dk, kvo, (256 * ch + 64 * w) * 8192 + col, LB + L1_UV + w * 8192);
#define KV_TOUCH() touch(mkd(ckv, 2 * 4096 * 1024), (lane >> 5) * 8192 + (lane & 31) * 128, ((xcd * 16 + r) * 64 + 16 * (w - 4) + 4) * 1024, LB + L_DUMP)
      const int cvo = (ob_row((w >> 1) & 1, h) ^ (CMIX ? (w & 2) << 12 : 0)) + i * 16384 + (w & 1) * 4096 + lane * 16;
      if (CLEAN_POS == 1) {
        bar0();
        if (w >= 4) {
          VMW(0);
#pragma unroll
          for (int k = 0; k < 4; ++k) stc(v4u{SENT, SENT, SENT, SENT}, rws, cvo + k * 1024);
          if (HELP_KV == 2) KV_TOUCH();
        }
      }
      ST(1);
      if (w < 4) VMW(8); else if (CLEAN_POS == 1) VMW(5); else if (w == 7) VMW(1); else VMW(0);
      v2u xf[5], f[20];
#pragma unroll
      for (int a = 0; a < 5; ++a) {
        xf[a] = *(LAS v2u*)(L + xr + a * 8);
#pragma unroll
        for (int t = 0; t < 4; ++t) f[a * 4 + t] = trd(L + ta[t] + a * 2048);
      }
      SCHED();
      f4 d[4];
#pragma unroll
      for (int t = 0; t < 4; ++t) d[t] = f4{0.f, 0.f, 0.f, 0.f};
#pragma unroll
      for (int a = 0; a < 5; ++a)
#pragma unroll
        for (int t = 0; t < 4; ++t) d[t] = mfma16(xf[a], f[a * 4 + t], d[t]);
      const u32 qq = pk((g & 1) ? d[2][0] : d[0][0], (g & 1) ? d[3][0] : d[1][0]);
      *(LAS u16*)(L + qw) = (u16)qq;
      *(LAS u16*)(L + qw + 32) = (u16)(qq >> 16);
      ST(2);
      SCHED();
      if (w < 4) {
        VMW(0);
#pragma unroll
        for (int k = 0; k < 5; ++k) {
          PIN(ga[k]);
          PIN(gc[k]);
        }
        v4u uk[8];
#pragma unroll
        for (int k = 0; k < 8; ++k) uk[k] = *(LAS v4u*)(L + (ur0 ^ ((k & 1) * 64 + (k & 4) * 8)) + (k >> 1) * 128);
        ST(12);
        bar();
        const v4u q0 = *(LAS v4u*)(L + qr), q1 = *(LAS v4u*)(L + qr + 64);
        f4 a[4];
#pragma unroll
        for (int t = 0; t < 4; ++t) a[t] = mfma(q0, uk[2 * t], f4{0.f, 0.f, 0.f, 0.f});
#pragma unroll
        for (int t = 0; t < 4; ++t) a[t] = mfma(q1, uk[2 * t + 1], a[t]);
        constexpr float SC = 0.08838834764831845f * 1.4426950408889634f;
        float v[4];
#pragma unroll
        for (int t = 0; t < 4; ++t) v[t] = x16_sum((a[t][0] + a[t][1]) + (a[t][2] + a[t][3])) * SC;
        stc2(v2u{pk(v[0], v[1]), pk(v[2], v[3])}, rws, svo, sso);
      } else {
        if (CLEAN_POS == 0) {
#pragma unroll
          for (int k = 0; k < 4; ++k) stc(v4u{SENT, SENT, SENT, SENT}, rws, cvo + k * 1024);
        }
        if (HELP_KV == 1) KV_TOUCH();
        if (HELP_KV) VMW(1); else VMW(0);
        ST(12);
        bar();
      }
    }
    bar0();
    ST(5);
    const int b = w >> 2, cb = w & 3;
    if (G1_DELAY) __builtin_amdgcn_s_sleep(G1_DELAY);
    ST(3);
    v2u vt;
    u32 xv;
    int x0, x1, x2;
    asm volatile("buffer_load_dwordx2 %0, %1, %2, %3 offen sc1" : "=v"(vt) : "v"(w == 1 && lane < 48 ? lane * 128 : OOB), "s"(dws), "s"(WS_VT) : "memory");
    if (WD_POS && w < 3) touch(mkd(wd, 1728 * 1280), w * 64 + lane < 160 ? lane * 128 : OOB, (xcd * 16 + r) * 20480 + w * 8192, LB + L_DUMP);
    {
      const int uvo = lr * 8192 + (((lane & 7) ^ (w & 6)) << 4), ovo = lr * 1280 + ckx;
      const int v4 = w & 3, uv4 = lr * 8192 + (((lane & 7) ^ (v4 << 1)) << 4), os4 = (h * 128 + 64 * dh + 16 * v4) * 1280;
      if (DMA4 == 1 && w >= 4) {
        dma_run2<4, 64 * 8192, 8192, USE_NT>(mkd(wuv, 512 * 8192), uv4, uv4 + 8 * 8192, (256 * ch + 16 * v4) * 8192 + col, LB + L1_UV + v4 * 2048);
        dma_run2<5, 128, 8192>(mkd(wo, 4096 * 1280), ovo, ovo + 8 * 1280, os4 + ch * 640, LB + L1_WO + ch * 40960 + v4 * 2048);
      }
      if (DMA4 == 2) {
        if (w >= 4) dma_run2<4, 64 * 8192, 8192, USE_NT>(mkd(wuv, 512 * 8192), uv4, uv4 + 8 * 8192, (256 * ch + 16 * v4) * 8192 + col, LB + L1_UV + v4 * 2048);
        else dma_run2<5, 128, 8192>(mkd(wo, 4096 * 1280), ovo, ovo + 8 * 1280, os4 + ch * 640, LB + L1_WO + ch * 40960 + v4 * 2048);
      }
      if (!DMA4) dma_run<4, 64 * 8192, 8192, PACE, USE_NT>(mkd(wuv, 512 * 8192), uvo, (256 * ch + 8 * w) * 8192 + col, LB + L1_UV + w * 1024);
      const v4u dwo = mkd(wo, 4096 * 1280);
      const int oso = (h * 128 + 64 * dh + 8 * w) * 1280;
      if (!DMA4) dma_run<5, 128, 8192, PACE>(dwo, ovo, oso + ch * 640, LB + L1_WO + ch * 40960 + w * 1024);
      if (DMA4 == 1) { if (w < 4) VMW(0); else VMW(18); }
      if (DMA4 == 2) { if (w < 4) VMW(10); else VMW(8); }
      if (DMA4) asm volatile("" : "+v"(vt)::"memory");
      else asm volatile("s_waitcnt vmcnt(9)" : "+v"(vt)::"memory");
      if (w == 1) *(LAS u32*)(L + L1_H) = count_votes(vt, tag, rws, lane);
      bar();
    xv = __builtin_amdgcn_readfirstlane(*(LAS u32*)(L + L1_H));
    x0 = xv & 0x2000, x1 = (xv >> 1) & 0x2000, x2 = (xv >> 2) & 0x2000;
    if (PRE_FA && w == 3) touch(dws, lane < 40 ? lane * 32768 : OOB, (WS_FA + FPAR + (h >> 4) * 16384 + ((h & 15) * 4 + i) * 128) ^ x0, LB + L_DUMP);
    if (PRE_FA && w == 4) touch(dws, lane < 40 ? lane * 128 : OOB, (WS_FF + FPAR + min(xcd * 16 + r, 107) * 16384) ^ x2, LB + L_DUMP);
      dma_run<5, 128, 8192>(dwo, ovo, oso + 640 - ch * 640, LB + L1_WO + 40960 - ch * 40960 + w * 1024);
    }
    ST(6);
    float gate = 0.f;
    if (w == 0) gate = mix_gate(ga, gc);
    PIN(gate);
    v4u sm = selmask(n);
    const int kg0 = 8 * g + 4 * (n >> 3);
    int vo = ob_kg(kg0) + (n & 7) * 16, so = (ob_row(b, h) + ch * 512 + cb * 128) ^ x1;
    int law = L1_LA + ((g & 2) + b) * 528 + cb * 128 + (n & 7) * 16 + (g & 1) * 8, lad = L_DUMP + lane * 8;
    PIN(law); PIN(lad);
    law = (g >> 1) == (n >> 3) ? law : lad;
    int lar = L1_LA + p * 528 + b * 256 + g * 16;
    int cxw = L1_CX + 2 * b * 144 + (32 * (cb >> 1) + 8 * q + 4 * (cb & 1) + p) * 2, cxr = L1_CX + p * 144 + g * 16;
    int fvo = g * 32768 + n * 8, fv4 = lane < 16 ? n * 8 + 4 * 32768 : OOB, fso = (WS_FA + FPAR + 5 * w * 32768 + (h >> 4) * 16384 + ((h & 15) * 4 + i) * 128) ^ x0;
    int wr = L1_W + b * 64 + kg0 * 2;
    int va = L1_UV + b * 16384 + (16 * q + g) * 128 + p * 8 + ((cb ^ q) << 5), to[5];
#pragma unroll
    for (int t = 0; t < 5; ++t) {
      const int T = 5 * w + t;
      to[t] = L1_WO + (T >> 2) * 8192 + rb + (((T & 3) ^ sw) << 5);
      PIN(to[t]);
    }
    int huo = lane < 8 ? WS_HU + (h * 4 + i) * 128 + lane * 16 : OOB;
    PIN(huo);
    PIN(sm); PIN(vo); PINS(so); PIN(law); PIN(lar); PIN(cxw); PIN(cxr); PIN(fvo); PIN(fv4); PINS(fso); PIN(wr); PIN(va);
    SCHED();
    DRAIN();
    ST(7);
    bar();
    v4u fv[4], fo[10];
    v4u ml;
    if (w == 0) ml = ldc(rws, ml_bk(lane), ml_h(h) ^ x0);
    SCHED();
    {
#pragma unroll
      for (int k = 0; k < 4; ++k) fv[k] = cat(trd(L + va + (k >> 1) * 8192 + (k & 1) * 512), trd(L + va + (k >> 1) * 8192 + (k & 1) * 512 + 1024));
#pragma unroll
      for (int t = 0; t < 5; ++t) {
        fo[2 * t] = cat(trd(L + to[t]), trd(L + to[t] + 2048));
        fo[2 * t + 1] = cat(trd(L + to[t] + 4096), trd(L + to[t] + 6144));
      }
    }
    SCHED();
    if (w == 0) {
#pragma unroll 1
      for (u32 it = 0; it < SPIN_CAP; ++it) {
        const u64 m1 = tagbad(ml, tag);
        if (!m1) break;
        reload1(ml, m1, ml_bk(lane), dws, ml_h(h) ^ x0);
      }
      ST(11);
    }
    bar0();
    v4u o0 = ldc(rws, vo, so), o1 = ldc(rws, vo + 1024, so), o2 = ldc(rws, vo + 2048, so), o3 = ldc(rws, vo + 3072, so);
    SCHED();
    if (w == 0) {
      const float m = __uint_as_float(ml.x);
      float M = fmaxf(m, dpp<XOR1>(m));
      M = fmaxf(M, dpp<XOR2>(M));
      M = fmaxf(M, dpp<ROR4>(M));
      M = fmaxf(M, dpp<ROR8>(M));
      M = x16_max(M);
      const float e = __builtin_amdgcn_exp2f(m - M);
      float Ls = e * __uint_as_float(ml.z);
      Ls += dpp<XOR1>(Ls);
      Ls += dpp<XOR2>(Ls);
      Ls += dpp<ROR4>(Ls);
      Ls += dpp<ROR8>(Ls);
      const float wn = e * gate * __builtin_amdgcn_rcpf(x16_sum(Ls));
      *(LAS u16*)(L + L1_W + lane * 2) = (u16)pk(wn, wn);
    }
    bar();
    const v2u w4 = *(LAS v2u*)(L + wr);
    v4u aw0 = sel(duplo(w4.x), sm), aw1 = sel(duphi(w4.x), sm), aw2 = sel(duplo(w4.y), sm), aw3 = sel(duphi(w4.y), sm);
    PIN(aw0); PIN(aw1); PIN(aw2); PIN(aw3);
    SCHED();
    {
      const u32 ma = max(max(max(max(o0.x, o0.y), o0.z), max(max(o0.w, o1.x), o1.y)), max(o1.z, o1.w));
      const u32 mb = max(max(max(max(o2.x, o2.y), o2.z), max(max(o2.w, o3.x), o3.y)), max(o3.z, o3.w));
      if (__builtin_expect(__builtin_amdgcn_ballot_w64(max(ma, mb) == SENT) != 0ull, 0)) {
#pragma unroll 1
        for (u32 it = 0; it < SPIN_CAP; ++it) {
          const u64 m0 = sentbad(o0), m1 = sentbad(o1), m2 = sentbad(o2), m3 = sentbad(o3);
          if (!(m0 | m1 | m2 | m3)) break;
          reload4(o0, o1, o2, o3, m0, m1, m2, m3, vo, dws, so);
        }
      }
    }
    ST(8);
    if (HEADSUP == 1 && w == 0) stc(v4u{tag, tag, tag, tag}, rws, huo);
    {
      f4 acc = mfma(aw0, o0, f4{0.f, 0.f, 0.f, 0.f});
      acc = mfma(aw1, o1, acc);
      acc = mfma(aw2, o2, acc);
      acc = mfma(aw3, o3, acc);
      *(LAS v2u*)(L + law) = v2u{pk(acc[0], acc[1]), pk(acc[2], acc[3])};
    }
    bar();
    if (HEADSUP == 2 && w == 0) stc(v4u{tag, tag, tag, tag}, rws, huo);
    {
      v4u a[4];
#pragma unroll
      for (int k = 0; k < 4; ++k) a[k] = *(LAS v4u*)(L + lar + k * 64);
      SCHED();
      f4 d = {0.f, 0.f, 0.f, 0.f};
#pragma unroll
      for (int k = 0; k < 4; ++k) d = mfma(a[k], fv[k], d);
      const u32 c = pk(d[0] + d[2], d[1] + d[3]);
      *(LAS u16*)(L + cxw) = (u16)c;
      *(LAS u16*)(L + cxw + 144) = (u16)(c >> 16);
    }
    bar();
    ST(9);
    {
      const v4u a0 = *(LAS v4u*)(L + cxr), a1 = *(LAS v4u*)(L + cxr + 64);
      u32 cv[5];
      f4 dacc[5];
#pragma unroll
      for (int t = 0; t < 5; ++t) dacc[t] = mfma(a0, fo[2 * t], f4{0.f, 0.f, 0.f, 0.f});
#pragma unroll
      for (int t = 0; t < 5; ++t) {
        dacc[t] = mfma(a1, fo[2 * t + 1], dacc[t]);
        cv[t] = pk(dacc[t][0] + dacc[t][2], dacc[t][1] + dacc[t][3]);
      }
      const u32 c01 = (g & 1) ? cv[1] : cv[0], c23 = (g & 1) ? cv[3] : cv[2];
      stc2(v2u{(g & 2) ? c23 : c01, tag}, rws, fvo, fso);
      stc2(v2u{cv[4], tag}, rws, fv4, fso);
    }
    ST(10);
    STEND();
    __builtin_amdgcn_endpgm();
  }
  const int j = r - 16, u = xcd * 16 + j, hg = r & 1, bk = u >> 1, b = bk >> 5, kg = bk & 31;
  int o_vo = ob_row(b, 16 * hg + 4 * g + 2 * (n >> 3)) + ob_kg(kg) + w * 128 + (n & 7) * 16, o_v1 = o_vo + 65536;
  if (j < 8 && (w == 2 || (w == 3 && hcol == 7168))) touch(mkd(wuv, 512 * 8192), bvo, bso - 1024, LB + L_DUMP);
  v4u d0, d1, d2, d3;
#define WD_LOAD()                                                                                                               \
  asm volatile("buffer_load_dwordx4 %0, %4, %5, %6 offen nt\n\tbuffer_load_dwordx4 %1, %4, %5, %6 offen offset:1280 nt\n\t"     \
               "buffer_load_dwordx4 %2, %4, %5, %6 offen offset:2560 nt\n\tbuffer_load_dwordx4 %3, %4, %5, %6 offen offset:3840 nt" \
               : "=&v"(d0), "=&v"(d1), "=&v"(d2), "=&v"(d3)                                                                     \
               : "v"(g * 5120 + n * 16), "s"(mkd(wd, 1728 * 1280)), "s"(w < 5 ? u * 20480 + w * 256 : OOB)                      \
               : "memory")
  u32 second = 0;
  if (w == 0 && j >= 10) {
    u64 t0, t1;
    const int pa = ((j - 10) >> 1) * 0x200000 + WS_PR + (j & 1) * 8192 + xcd * 128;
    __builtin_amdgcn_s_sleep(PR_DELAY);
    asm volatile("s_memtime %0\n\tbuffer_store_dwordx4 %2, %3, %4, %5 offen sc1\n\ts_waitcnt vmcnt(0)\n\ts_memtime %1\n\ts_waitcnt lgkmcnt(0)"
                 : "=&s"(t0), "=&s"(t1)
                 : "v"(v4u{SENT, SENT, SENT, SENT}), "v"(lane < 8 ? lane * 16 : OOB), "s"(dws), "s"(pa)
                 : "memory");
    second = (u32)t1 - (u32)t0;
  } else if (j >= 10 || !WD_EARLY) {
    __builtin_amdgcn_s_sleep(UK_DELAY);
  }
  if (WD_EARLY) {
    WD_LOAD();
    if (j < 10) __builtin_amdgcn_s_sleep(UK_DELAY);
  }
  GET_TAG();
  if (w == 0 && j >= 10) stc2(v2u{second * 2u + ((j ^ (xcd >> 2)) & 1u), tag}, rws, lane == 0 ? WS_VT + ((j - 10) * 8 + xcd) * 128 : OOB, 0);
  touch(dk, tvo, tso, LB + L_DUMP);
  bar0();
  if (G2_DELAY) __builtin_amdgcn_s_sleep(G2_DELAY);
  ST(1);
  v2u f1;
  {
    const v4u dc = mkd(ckv, 2 * 4096 * 1024);
    const int c0 = (bk * 128 + 64 * hg + 8 * w) * 1024, c1 = (bk * 128 + 64 - 64 * hg + 8 * w) * 1024, cl = LB + 16 * w * RS;
    if (HELP_KV) {
      dma_run<4, 1024, RS>(dc, lane * 16, c0, cl);
      bar0();
      dma_run<4, 1024, RS>(dc, lane * 16, c1, cl + 8 * RS);
      if (KV_WAIT) VMW(0);
      bar0();
      if (KV_WAIT > 1) __builtin_amdgcn_s_sleep(KV_WAIT);
    asm volatile("buffer_load_dwordx2 %0, %1, %2, %3 offen sc1" : "=v"(f1) : "v"(lane < 64 - 16 * w - (w > 1 ? 64 : 0) ? lane * 128 : OOB), "s"(dws), "s"(w == 0 ? WS_F1 + hg * 8192 : WS_VT) : "memory");
      dma_run<4, 1024, RS>(dc, lane * 16, c0 + 4096, cl + 4 * RS);
      dma_run<4, 1024, RS>(dc, lane * 16, c1 + 4096, cl + 12 * RS);
    } else {
      dma_run<8, 1024, RS>(dc, lane * 16, c0, cl);
      bar0();
    asm volatile("buffer_load_dwordx2 %0, %1, %2, %3 offen sc1" : "=v"(f1) : "v"(lane < 64 - 16 * w - (w > 1 ? 64 : 0) ? lane * 128 : OOB), "s"(dws), "s"(w == 0 ? WS_F1 + hg * 8192 : WS_VT) : "memory");
      dma_run<8, 1024, RS>(dc, lane * 16, c1, cl + 8 * RS);
    }
  }
  int qfw = L2_QF + 2 * w * RS + lane * 16, qfr = L2_QF + n * RS + g * 16, mxw = L2_MX + n * 80 + w * 8 + (lane >> 5) * 4, mxr = L2_MX + n * 80;
  int pfw = L2_PF + (w >> 1) * 1024 + lane * 16 + (w & 1) * 8, pfr = L2_PF + lane * 16;
  int mlo = (ml_bk(bk) + ml_h(16 * hg + n)) | (lane < 16 ? 0 : OOB);
  int vr0 = (4 * g + (n >> 2)) * RS + w * 128 + (n & 3) * 8, vr1 = vr0 + 64 * RS, kr = (16 * w + n) * RS + g * 16;
  PIN(o_vo); PIN(o_v1); PIN(qfw); PIN(qfr); PIN(mxw); PIN(mxr); PIN(pfw); PIN(pfr); PIN(mlo); PIN(vr0); PIN(vr1); PIN(kr);
  const int frow = 160 * (j & 3) + 20 * w + 5 * g;
  const GAS unsigned char* wp = (const GAS unsigned char*)(n < 8 ? wg : wu) + (frow + 1) * 3456 + (u < 108 ? u >> 2 : 0) * 128 + (n & 7) * 16;
  const GAS unsigned char* wp2 = wp + 3 * 3456;
  asm volatile("" : "+v"(wp), "+v"(wp2));
  ST(2);
  v4u kf[16], vf[16], wv[5];
  int xo = ((n >> 3) * 1280 + frow * 2) & ~3;
  PIN(xo);
  v3u xd;
  u32 xv;
#define LOAD_GU(t)                                                                      \
  {                                                                                     \
    const GAS v4u* a = (const GAS v4u*)(((t) < 3 ? wp : wp2) + ((t) % 3 - 1) * 3456);   \
    wv[t] = USE_NT ? __builtin_nontemporal_load(a) : *a;                                \
  }
  {
    const int vo = lane * 16, so = qa_row(0, b, 16 * hg + 2 * w), sb = qa_row(1, b, 16 * hg + 2 * w);
    if (w == 0) {
      asm volatile("s_waitcnt vmcnt(8)" : "+v"(f1)::"memory");
      wait_flags(f1.x, tag, rws, lane, WS_F1 + hg * 8192);
    }
    bar0();
    if (QG_DELAY) __builtin_amdgcn_s_sleep(QG_DELAY);
    v4u q0 = ldc(rws, vo, so), q1 = ldc(rws, vo + 1024, so), q2 = ldc(rws, vo, sb), q3 = ldc(rws, vo + 1024, sb);
    SCHED();
    asm volatile("s_waitcnt vmcnt(4)" : "+v"(f1)::"memory");
    if (WD_EARLY) asm volatile("" : "+v"(d0), "+v"(d1), "+v"(d2), "+v"(d3));
    ST(3);
#pragma unroll
    for (int s = 0; s < 16; ++s) kf[s] = *(LAS v4u*)(L + kr + s * 64);
    SCHED();
    if (w == 1) *(LAS u32*)(L + L2_H) = count_votes(f1, tag, rws, lane);
    settle22(q0, q1, q2, q3, vo, dws, so, sb);
    ST(4);
    *(LAS v4u*)(L + qfw) = v4u{addbf(q0.x, q2.x), addbf(q0.y, q2.y), addbf(q0.z, q2.z), addbf(q0.w, q2.w)};
    *(LAS v4u*)(L + qfw + RS) = v4u{addbf(q1.x, q3.x), addbf(q1.y, q3.y), addbf(q1.z, q3.z), addbf(q1.w, q3.w)};
  }
  xd = __builtin_amdgcn_raw_buffer_load_b96(mk(x, 2560), xo, 0, 0);
#pragma unroll
  for (int t = 0; t < GU_EARLY; ++t) LOAD_GU(t);
  SCHED();
  ST(5);
  bar();
  f4 sa = {0.f, 0.f, 0.f, 0.f};
  {
    v4u qf[16];
#pragma unroll
    for (int s = 0; s < 16; ++s) qf[s] = *(LAS v4u*)(L + qfr + s * 64);
    SCHED();
#define VFR(s) _Pragma("unroll") for (int t = 0; t < 4; ++t) \
      vf[(s) * 4 + t] = cat(trd(L + ((s) < 2 ? vr0 : vr1) + ((s) & 1) * 32 * RS + t * 32), trd(L + ((s) < 2 ? vr0 : vr1) + (((s) & 1) * 32 + 16) * RS + t * 32))
    xv = *(LAS u32*)(L + L2_H);
    if (VF_EARLY) {
      VFR(0);
      SCHED();
    }
#pragma unroll
    for (int s = 0; s < 16; ++s) {
      sa = mfma(kf[s], qf[s], sa);
      if (s % 3 == 0 && s / 3 + GU_EARLY < 5) {
        LOAD_GU(s / 3 + GU_EARLY);
        SCHED();
      }
    }
  }
  xv = __builtin_amdgcn_readfirstlane(xv);
  o_vo ^= (xv >> 1) & 0x2000;
  o_v1 ^= (xv >> 1) & 0x2000;
  mlo ^= xv & 0x2000;
  PIN(o_vo); PIN(o_v1); PIN(mlo); PINS(xv);
  {
    float m = fmaxf(fmaxf(sa[0], sa[1]), fmaxf(sa[2], sa[3]));
    auto s1 = __builtin_amdgcn_permlane16_swap(__float_as_uint(m), __float_as_uint(m), false, false);
    m = max2(__uint_as_float(s1[0]), __uint_as_float(s1[1]));
    *(LAS float*)(L + mxw) = m;
  }
  ST(6);
  bar();
  float M;
  {
    const f4 m0 = *(LAS f4*)(L + mxr), m1 = *(LAS f4*)(L + mxr + 16), m2 = *(LAS f4*)(L + mxr + 32), m3 = *(LAS f4*)(L + mxr + 48);
    SCHED();
    if (!VF_EARLY) { VFR(0); }
    VFR(1);
    SCHED();
    M = max3(max3(max3(m0[0], m0[1], m0[2]), max3(m0[3], m1[0], m1[1]), max3(m1[2], m1[3], m2[0])), max3(m2[1], m2[2], m2[3]),
             max2(max3(m3[0], m3[1], m3[2]), m3[3]));
    const u32 p01 = pk(__builtin_amdgcn_exp2f(sa[0] - M), __builtin_amdgcn_exp2f(sa[1] - M));
    const u32 p23 = pk(__builtin_amdgcn_exp2f(sa[2] - M), __builtin_amdgcn_exp2f(sa[3] - M));
    *(LAS v2u*)(L + pfw) = v2u{p01, p23};
  }
  bar();
  ST(7);
  {
    v4u pf[4];
#pragma unroll
    for (int s = 0; s < 4; ++s) pf[s] = *(LAS v4u*)(L + pfr + s * 1024);
    SCHED();
    VFR(2);
    VFR(3);
    SCHED();
    if (w == 0) {
      const v4u one = {0x3F803F80u, 0x3F803F80u, 0x3F803F80u, 0x3F803F80u};
      f4 l = {0.f, 0.f, 0.f, 0.f};
#pragma unroll
      for (int s = 0; s < 4; ++s) l = mfma(one, pf[s], l);
      stc(v4u{__float_as_uint(M), tag, __float_as_uint(l[0]), tag}, rws, mlo);
    }
    SCHED();
    f4 o[4];
#pragma unroll
    for (int t = 0; t < 4; ++t) o[t] = f4{0.f, 0.f, 0.f, 0.f};
#pragma unroll
    for (int s = 0; s < 4; ++s)
#pragma unroll
      for (int t = 0; t < 4; ++t) o[t] = mfma(pf[s], vf[s * 4 + t], o[t]);
    u32 pa[4], pb[4];
#pragma unroll
    for (int k = 0; k < 4; ++k) {
      pa[k] = pk(o[0][k], o[1][k]);
      pb[k] = pk(o[2][k], o[3][k]);
    }
#pragma unroll
    for (int k = 0; k < 2; ++k) {
      v4u pc;
      pc.x = __builtin_amdgcn_update_dpp(pa[k], pa[2 + k], ROR8, 0xf, 0xc, false);
      pc.y = __builtin_amdgcn_update_dpp(pb[k], pb[2 + k], ROR8, 0xf, 0xc, false);
      pc.z = __builtin_amdgcn_update_dpp(pa[2 + k], pa[k], ROR8, 0xf, 0x3, false);
      pc.w = __builtin_amdgcn_update_dpp(pb[2 + k], pb[k], ROR8, 0xf, 0x3, false);
      stc(pc, rws, k ? o_v1 : o_vo);
    }
  }
  ST(8);
  const int x0 = xv & 0x2000, x2 = (xv >> 2) & 0x2000;
  if (u < 108) {
    if (!WD_EARLY) {
      bar0();
      const rsrc_t rd = mk(wd, 1728 * 1280);
      const int so = w < 5 ? u * 20480 + w * 256 : OOB, dvo = g * 4 * 1280 + n * 16;
      d0 = __builtin_amdgcn_raw_buffer_load_b128(rd, dvo, so, 2), d1 = __builtin_amdgcn_raw_buffer_load_b128(rd, dvo + 1280, so, 2);
      d2 = __builtin_amdgcn_raw_buffer_load_b128(rd, dvo + 2560, so, 2), d3 = __builtin_amdgcn_raw_buffer_load_b128(rd, dvo + 3840, so, 2);
      SCHED();
    }
    const v4u sm = selmask(n);
    const u32 sh = (g & 1) << 4;
    const u32 a0 = __builtin_amdgcn_alignbit(xd.y, xd.x, sh), a1 = __builtin_amdgcn_alignbit(xd.z, xd.y, sh), a2 = xd.z >> sh;
    f4 acc = mfma(sel(duplo(a0), sm), wv[0], f4{0.f, 0.f, 0.f, 0.f});
    acc = mfma(sel(duphi(a0), sm), wv[1], acc);
    acc = mfma(sel(duplo(a1), sm), wv[2], acc);
    acc = mfma(sel(duphi(a1), sm), wv[3], acc);
    acc = mfma(sel(duplo(a2), sm), wv[4], acc);
    *(LAS f4*)(L + L2_QF + w * 1024 + lane * 16) = acc;
    bar();
    if (w == 6) {
      v2u ga[5], gc[5];
      gate_load(ga, gc, x, gw, lane);
      *(LAS float*)(L + L2_MX + (lane >> 5) * 4) = 1.f - mix_gate(ga, gc);
    }
    if (w == 7) {
      f4 e[8];
#pragma unroll
      for (int k = 0; k < 8; ++k) e[k] = *(LAS f4*)(L + L2_QF + k * 1024 + lane * 16);
      SCHED();
      f4 s = ((e[0] + e[1]) + (e[2] + e[3])) + ((e[4] + e[5]) + (e[6] + e[7]));
      ST(9);
      s = exchange4(s, rws, dws, (WS_QP + ((xcd & 3) * 2 + (j >> 3)) * 16384 + ((xcd & 4) << 11) + (j & 4) * 1024) ^ x0, j & 3, lane, tag);
      float hv[4];
#pragma unroll
      for (int k = 0; k < 4; ++k) hv[k] = s[k] * dpp<ROR8>(s[k]) * __builtin_amdgcn_rcpf(1.f + __expf(-s[k]));
      *(LAS v2u*)(L + L2_H + (g >> 1) * 256 + n * 16 + (g & 1) * 8) = v2u{pk(hv[0], hv[1]), pk(hv[2], hv[3])};
      ST(10);
    }
    int hr = L2_H + (n >> 3) * 256 + (j & 3) * 32 + g * 8, fo = ((WS_FF + FPAR + u * 16384) ^ x2) + w * 512 + (g >> 1) * 2560 + n * 32 + (g & 1) * 16;
    PIN(hr); PIN(fo);
    bar();
    if (w < 5) {
      const v2u hh = *(LAS v2u*)(L + hr);
      const float og = *(LAS float*)(L + L2_MX + (lane >> 5) * 4);
      f4 d = mfma(sel(duplo(hh.x), sm), d0, f4{0.f, 0.f, 0.f, 0.f});
      d = mfma(sel(duphi(hh.x), sm), d1, d);
      d = mfma(sel(duplo(hh.y), sm), d2, d);
      d = mfma(sel(duphi(hh.y), sm), d3, d);
      d *= og;
      stc(v4u{pk(d[0], d[1]), tag, pk(d[2], d[3]), tag}, rws, fo);
    }
    ST(11);
  }
  if (EP_EARLY && u == 0 && w == 5) {
#pragma unroll 1
    for (int k = 0; k < 2; ++k) {
      v4u a = ldc(rws, ml_bk(lane), WS_ML + k * 128);
#pragma unroll 1
      for (u32 it = 0; it < SPIN_CAP; ++it) {
        const u64 m1 = tagbad(a, tag);
        if (!m1) break;
        __builtin_amdgcn_s_sleep(4);
        reload1(a, m1, ml_bk(lane), dws, WS_ML + k * 128);
      }
    }
    stc1(epoch, rws, lane == 0 ? WS_EPOCH : OOB, 0);
  }
  if (u >= 88) {
    const int T = u - 88;
    int v0, v3, st, so;
    if (w < 4) {
      so = (WS_FA + FPAR + T * 32768 + (w >> 1) * 16384 + (w & 1) * 4096) ^ x0;
      v0 = lane * 16;
      st = 1024;
      v3 = v0 + 3072;
    } else {
      so = (WS_FF + FPAR + T * 64 + (w - 4) * 27 * 16384) ^ x2;
      v0 = (lane >> 3) * 16384 + ((lane >> 2) & 1) * 2560 + (lane & 3) * 16;
      st = 8 * 16384;
      v3 = lane < 24 ? v0 + 24 * 16384 : v0;
    }
    int v1 = v0 + st, v2 = v1 + st;
    int fw = L2_FR + w * 128 + (lane & 7) * 16;
    int fr = L2_FR + n * 8 + (g & 1) * 4, fr2 = L2_FR + 512 + (g & 1) * 64 + n * 4;
    PIN(fr); PIN(fr2);
    fr = (lane & 32) ? fr2 : fr;
    int ovo = ((lane & 32) << 25) | ((g & 1) * 1280 + n * 2 + T * 32);
    u32 keep = (w >= 4 && lane >= 24) ? 0u : ~0u;
    const u32 o1 = 0x3F80u << ((n & 1) << 4);
    v4u pick = {(n & 2) ? 0u : o1, 0u, (n & 2) ? o1 : 0u, 0u};
    PIN(v0); PIN(v1); PIN(v2); PIN(v3); PIN(fw); PIN(fr); PIN(ovo); PIN(pick); PIN(keep);
    const rsrc_t ro = mk(out, 2560);
    if (FA_REP) {
      const int rso = HEADSUP && w < 4 ? WS_HU + (32 * w + (T & 31)) * 128 : so;
      v4u a = ldc(rws, 0, rso);
#pragma unroll 1
      for (u32 it = 0; it < SPIN_CAP; ++it) {
        if (!tagbad(a, tag)) break;
        reload1(a, ~0ull, 0, dws, rso);
      }
    }
    ST(12);
    asm volatile("" ::: "memory");
    v4u a0 = ldc(rws, v0, so), a1 = ldc(rws, v1, so), a2 = ldc(rws, v2, so), a3 = ldc(rws, v3, so);
    if (__builtin_expect(!alltag(tag, a0, a1, a2, a3), 0)) {
#pragma unroll 1
      for (u32 it = 0; it < SPIN_CAP; ++it) {
        const u64 m0 = tagbad(a0, tag), m1 = tagbad(a1, tag), m2 = tagbad(a2, tag), m3 = tagbad(a3, tag);
        if (!(m0 | m1 | m2 | m3)) break;
        reload4v(a0, a1, a2, a3, m0, m1, m2, m3, v0, v1, v2, v3, dws, so);
      }
    }
    ST(13);
    a3.x &= keep;
    a3.z &= keep;
    f4 v = mfma(pick, a0, f4{0.f, 0.f, 0.f, 0.f});
    v = mfma(pick, a1, v);
    v = mfma(pick, a2, v);
    v = mfma(pick, a3, v);
#pragma unroll
    for (int k = 0; k < 4; ++k) v[k] += dpp<ROR8>(v[k]);
    *(LAS f4*)(L + fw) = v;
    bar();
    if (w == 0) {
      float A = (*(LAS float*)(L + fr) + *(LAS float*)(L + fr + 128)) + (*(LAS float*)(L + fr + 256) + *(LAS float*)(L + fr + 384));
      auto t = __builtin_amdgcn_permlane32_swap(__float_as_uint(A), __float_as_uint(A), false, false);
      A = __uint_as_float(t[0]) + __uint_as_float(t[1]);
      __builtin_amdgcn_raw_buffer_store_b16((u16)pk(A, A), ro, ovo, 0, OUT_AUX);
      if (!EP_EARLY && T == 0) stc1(epoch, rws, lane == 0 ? WS_EPOCH : OOB, 0);
    }
    ST(14);
  }
  STEND();
}

extern "C" void kernel_launch(void* const* d_in, const int* in_sizes, int n_in, void* d_out, int out_size, void* d_ws, size_t ws_size,
                              hipStream_t stream) {
  mla_ffn_mix<<<256, 512, 0, stream>>>((const u16*)d_in[0], (const u16*)d_in[1], (const u16*)d_in[2], (const u16*)d_in[3],
                                       (const u16*)d_in[4], (const u16*)d_in[5], (const u16*)d_in[7], (const u16*)d_in[8],
                                       (const u16*)d_in[9], (const u16*)d_in[10], (u16*)d_out, (unsigned char*)d_ws);
}
```
